```python
import torch

def _softmax(x, axis=-1):
    return torch.softmax(x, dim=axis)
def _gelu(x):
    return torch.nn.functional.gelu(x, approximate="tanh")
def _silu(x):
    return torch.nn.functional.silu(x)
def _sigmoid(x):
    return torch.sigmoid(x)
def _layernorm(x, axis=-1, eps=1e-5):
    m = x.mean(dim=axis, keepdim=True)
    v = x.var(dim=axis, keepdim=True, unbiased=False)
    return (x - m) / torch.sqrt(v + eps)
def _scatter_add(tgt, idx, src, axis=0):
    return tgt.clone().index_add_(axis, idx, src)
def _segment_sum(src, seg, n):
    out = torch.zeros((n,) + src.shape[1:], dtype=src.dtype)
    return out.index_add_(0, seg, src)
def _pad(x, pw):
    flat = tuple(v for pair in reversed(pw) for v in pair)
    return torch.nn.functional.pad(x, flat)

kernel_name = 'recurrent_scan__spectral_gfnet__glu_gated_swiglu__prompt'

def setup_inputs():

    _w = (lambda a: (lambda t: t.to(torch.bfloat16) if t.is_floating_point() else t)(torch.as_tensor(a)))
    def _A_setup():
        B, S, D = 1, 2048, 640
        Df = D // 2 + 1
        return {
            "x": _w((lambda *s: torch.randn(*s, dtype=torch.bfloat16) * 0.2)(B, S, D)),
            "w_r": _w((lambda *s: torch.randn(*s, dtype=torch.bfloat16) * 1.6)(S, Df) * 0.02),
            "w_i": _w((lambda *s: torch.randn(*s, dtype=torch.bfloat16) * 1.6)(S, Df) * 0.02),
        }
    _A_in = _A_setup()

    def _B_setup():
        B, S, D, F = 1, 2048, 640, 1536
        return {
            "x": _w((lambda *s: torch.randn(*s, dtype=torch.bfloat16) * 0.2)(B, S, D)),
            "w1": _w((lambda *s: torch.randn(*s, dtype=torch.bfloat16) * 0.8)(D, F) * (2.0 / D) ** 0.5),
            "w3": _w((lambda *s: torch.randn(*s, dtype=torch.bfloat16) * 0.8)(D, F) * (2.0 / D) ** 0.5),
            "w2": _w((lambda *s: torch.randn(*s, dtype=torch.bfloat16) * 0.8)(F, D) * (2.0 / F) ** 0.5),
        }
    _B_in = _B_setup()

    return {
        "A_x": _A_in["x"],
        "A_w_r": _A_in["w_r"],
        "A_w_i": _A_in["w_i"],
        "B_x": _B_in["x"],
        "B_w1": _B_in["w1"],
        "B_w3": _B_in["w3"],
        "B_w2": _B_in["w2"],
    }

def reference(A_x, A_w_r, A_w_i, B_x, B_w1, B_w3, B_w2):
    def _A_ref(x, w_r, w_i):
        B, S, D = x.shape
        X = (lambda x, axes=(-2,-1): torch.fft.rfft2(x.float(), dim=axes))(x, axes=(-2, -1))
        W = w_r + 1j * w_i
        X = X * W
        y = (lambda x, s, axes=(-2,-1): torch.fft.irfft2(x, s=s, dim=axes).to(torch.bfloat16))(X, (S, D), axes=(-2, -1))
        return y

    def _B_ref(x, w1, w3, w2):
        gate = torch.einsum("bsd,df->bsf", x, w1)
        up = torch.einsum("bsd,df->bsf", x, w3)
        h = _silu(gate) * up
        out = torch.einsum("bsf,fd->bsd", h, w2)
        return out

    x = A_x
    xn = _layernorm(x, axis=-1)
    s = _A_ref(xn, A_w_r, A_w_i)
    g = _silu(_B_ref(xn, B_w1, B_w3, B_w2))
    return x + s * g
```

```cpp
#include <hip/hip_runtime.h>
#include <hip/hip_bf16.h>
#include <stdint.h>

typedef float f32x2 __attribute__((ext_vector_type(2)));
typedef float f32x4 __attribute__((ext_vector_type(4)));
typedef short s16x4 __attribute__((ext_vector_type(4)));
typedef short s16x8 __attribute__((ext_vector_type(8)));
typedef __bf16 bf16x2 __attribute__((ext_vector_type(2)));
typedef __bf16 bf16x8 __attribute__((ext_vector_type(8)));
typedef unsigned u32x2 __attribute__((ext_vector_type(2)));
typedef unsigned u32x4 __attribute__((ext_vector_type(4)));

#define DEV __device__ __forceinline__
#define LAS __attribute__((address_space(3)))
#if defined(__HIP_DEVICE_COMPILE__)
#define NOPK __attribute__((target("no-packed-fp32-ops")))
#else
#define NOPK
#endif

constexpr int S_ = 2048, D_ = 640, F_ = 1536, DF_ = 321;
constexpr int NG4 = 81;
constexpr int NUNIT = 8 * NG4;

constexpr size_t WS_XN = 0;
constexpr size_t WS_T = 4u << 20;
constexpr size_t WS_WT = 12u << 20;
constexpr size_t WS_W2T = 16u << 20;
constexpr size_t WS_HT = 20u << 20;
constexpr size_t WS_S = 28u << 20;
constexpr size_t WS_FLAG = 32u << 20;

struct cf { float x, y; };
DEV cf operator+(cf a, cf b) { return {a.x + b.x, a.y + b.y}; }
DEV cf operator-(cf a, cf b) { return {a.x - b.x, a.y - b.y}; }
DEV cf cmul(cf a, cf w) { return {a.x * w.x - a.y * w.y, a.x * w.y + a.y * w.x}; }
DEV cf cmulc(cf a, cf w) { return {a.x * w.x + a.y * w.y, a.y * w.x - a.x * w.y}; }
template <int DIR> DEV cf muli(cf a) { return DIR > 0 ? cf{-a.y, a.x} : cf{a.y, -a.x}; }
DEV cf twid(float f) { return {__builtin_amdgcn_cosf(f), -__builtin_amdgcn_sinf(f)}; }

DEV unsigned pack_bf16(float a, float b) {
    f32x2 v = {a, b};
    return __builtin_bit_cast(unsigned, __builtin_convertvector(v, bf16x2));
}
DEV float bf_lo(unsigned u) { return __uint_as_float(u << 16); }
DEV float bf_hi(unsigned u) { return __uint_as_float(u & 0xffff0000u); }
DEV float rbf(float a) { return bf_lo(pack_bf16(a, a)); }

template <int DIR> DEV void fft4(cf& a0, cf& a1, cf& a2, cf& a3) {
    cf t0 = a0 + a2, t1 = a0 - a2, t2 = a1 + a3, t3 = muli<DIR>(a1 - a3);
    a0 = t0 + t2; a2 = t0 - t2; a1 = t1 + t3; a3 = t1 - t3;
}
constexpr float RH = 0.70710678118654752f;
template <int DIR> DEV cf mulw8(cf a) { return DIR > 0 ? cf{(a.x - a.y) * RH, (a.x + a.y) * RH} : cf{(a.x + a.y) * RH, (a.y - a.x) * RH}; }
template <int DIR> DEV cf mulw83(cf a) { return DIR > 0 ? cf{-(a.x + a.y) * RH, (a.x - a.y) * RH} : cf{(a.y - a.x) * RH, -(a.x + a.y) * RH}; }

template <int DIR> DEV void fft8(cf (&a)[8]) {
    fft4<DIR>(a[0], a[2], a[4], a[6]);
    fft4<DIR>(a[1], a[3], a[5], a[7]);
    cf o1 = mulw8<DIR>(a[3]), o2 = muli<DIR>(a[5]), o3 = mulw83<DIR>(a[7]);
    cf e0 = a[0], e1 = a[2], e2 = a[4], e3 = a[6], o0 = a[1];
    a[0] = e0 + o0; a[4] = e0 - o0;
    a[1] = e1 + o1; a[5] = e1 - o1;
    a[2] = e2 + o2; a[6] = e2 - o2;
    a[3] = e3 + o3; a[7] = e3 - o3;
}
template <int DIR> DEV void fft5(cf (&a)[5]) {
    constexpr float c1 = 0.30901699437494742f, c2 = -0.80901699437494742f;
    constexpr float s1 = 0.95105651629515357f, s2 = 0.58778525229247313f;
    cf t1 = a[1] + a[4], t2 = a[2] + a[3], t3 = a[1] - a[4], t4 = a[2] - a[3];
    cf m1 = {a[0].x + c1 * t1.x + c2 * t2.x, a[0].y + c1 * t1.y + c2 * t2.y};
    cf m2 = {a[0].x + c2 * t1.x + c1 * t2.x, a[0].y + c2 * t1.y + c1 * t2.y};
    cf n1 = muli<DIR>(cf{s1 * t3.x + s2 * t4.x, s1 * t3.y + s2 * t4.y});
    cf n2 = muli<DIR>(cf{s2 * t3.x - s1 * t4.x, s2 * t3.y - s1 * t4.y});
    a[0] = a[0] + t1 + t2;
    a[1] = m1 + n1; a[4] = m1 - n1;
    a[2] = m2 + n2; a[3] = m2 - n2;
}
template <int DIR> DEV void fft16_a(cf (&a)[16]) {
    constexpr float C1 = 0.92387953251128674f, S1 = 0.38268343236508977f;
#pragma unroll
    for (int n2 = 0; n2 < 4; ++n2) fft4<DIR>(a[n2], a[n2 + 4], a[n2 + 8], a[n2 + 12]);
    const cf w1 = {C1, DIR * S1}, w3 = {S1, DIR * C1}, w9 = {-C1, -DIR * S1};
    a[1 + 4] = cmul(a[1 + 4], w1);
    a[1 + 8] = mulw8<DIR>(a[1 + 8]);
    a[1 + 12] = cmul(a[1 + 12], w3);
    a[2 + 4] = mulw8<DIR>(a[2 + 4]);
    a[2 + 8] = muli<DIR>(a[2 + 8]);
    a[2 + 12] = mulw83<DIR>(a[2 + 12]);
    a[3 + 4] = cmul(a[3 + 4], w3);
    a[3 + 8] = mulw83<DIR>(a[3 + 8]);
    a[3 + 12] = cmul(a[3 + 12], w9);
}
template <int DIR> DEV void fft16_b(cf (&a)[16]) {
#pragma unroll
    for (int k1 = 0; k1 < 4; ++k1) fft4<DIR>(a[4 * k1], a[4 * k1 + 1], a[4 * k1 + 2], a[4 * k1 + 3]);
    cf b[16];
#pragma unroll
    for (int k1 = 0; k1 < 4; ++k1)
#pragma unroll
        for (int k2 = 0; k2 < 4; ++k2) b[k1 + 4 * k2] = a[4 * k1 + k2];
#pragma unroll
    for (int i = 0; i < 16; ++i) a[i] = b[i];
}

template <int CTRL, int RM> DEV float dpp_add(float v) {
    int t = __builtin_amdgcn_update_dpp(0, __float_as_int(v), CTRL, RM, 0xf, true);
    return v + __int_as_float(t);
}
DEV float wave_sum(float v) {
    v = dpp_add<0x111, 0xf>(v);
    v = dpp_add<0x112, 0xf>(v);
    v = dpp_add<0x114, 0xf>(v);
    v = dpp_add<0x118, 0xf>(v);
    v = dpp_add<0x142, 0xa>(v);
    v = dpp_add<0x143, 0xc>(v);
    return __int_as_float(__builtin_amdgcn_readlane(__float_as_int(v), 63));
}
DEV float rdlane(float v, int l) { return __int_as_float(__builtin_amdgcn_readlane(__float_as_int(v), l)); }
DEV void wave_fence() { asm volatile("" ::: "memory"); __builtin_amdgcn_wave_barrier(); }

DEV cf lds_ld(const char* base, int idx) { f32x2 v = *(const LAS f32x2*)((LAS const char*)base + idx * 8); return {v.x, v.y}; }
DEV void lds_st(char* base, int idx, cf a) { f32x2 v = {a.x, a.y}; *(LAS f32x2*)((LAS char*)base + idx * 8) = v; }

constexpr int ROWBUF = 360;
constexpr int K1_WT_BLOCKS = 128, K1_W2_BLOCKS = 120;

__global__ __launch_bounds__(512, 2) void k1_kernel(const unsigned* __restrict__ x32, const unsigned short* __restrict__ wr,
                                                   const unsigned short* __restrict__ wi, const unsigned short* __restrict__ w2,
                                                   char* __restrict__ ws) {
    __shared__ __attribute__((aligned(16))) char smem[8 * ROWBUF * 8];
    const int b = blockIdx.x, tid = threadIdx.x, lane = tid & 63;
    const int wv = __builtin_amdgcn_readfirstlane(tid >> 6);
    if (b < 256) {
        const int s0 = (b & 7) * 32 + (b >> 3);
        const int s = s0 + 256 * wv;
        unsigned z[5];
#pragma unroll
        for (int i = 0; i < 5; ++i) z[i] = x32[s * 320 + lane + 64 * i];
        if (b == 0 && tid < 256) ((unsigned*)(ws + WS_FLAG))[tid] = 0u;
        cf tw5[5], tw8[8];
#pragma unroll
        for (int p = 1; p < 5; ++p) tw5[p] = twid((float)(lane * p) * (1.0f / 320.0f));
        const int pp = lane >> 3, t1 = lane & 7;
#pragma unroll
        for (int a = 1; a < 8; ++a) tw8[a] = twid((float)(t1 * a) * (1.0f / 64.0f));
        cf twk = twid((float)tid * (1.0f / 640.0f));
        twk = muli<-1>(twk);
        cf twq = twid((float)(s0 * (lane & 7)) * (1.0f / 2048.0f));
        float v[10];
#pragma unroll
        for (int i = 0; i < 5; ++i) { v[2 * i] = bf_lo(z[i]); v[2 * i + 1] = bf_hi(z[i]); }
        const float c0 = rdlane(v[0], 0);
        float s1 = 0.f, s2 = 0.f;
#pragma unroll
        for (int i = 0; i < 10; ++i) { float d = v[i] - c0; s1 += d; s2 += d * d; }
        s1 = wave_sum(s1); s2 = wave_sum(s2);
        const float md = s1 * (1.0f / 640.0f);
        float var = s2 * (1.0f / 640.0f) - md * md;
        var = var < 0.f ? 0.f : var;
        const float m = rbf(c0 + md);
        const float sd = rbf(__builtin_sqrtf(rbf(rbf(var) + 1e-5f)));
        const float rs = 1.0f / sd;
        cf a5[5];
        unsigned* xn32 = (unsigned*)(ws + WS_XN);
#pragma unroll
        for (int i = 0; i < 5; ++i) {
            unsigned d = pack_bf16(v[2 * i] - m, v[2 * i + 1] - m);
            unsigned n = pack_bf16(bf_lo(d) * rs, bf_hi(d) * rs);
            xn32[s * 320 + lane + 64 * i] = n;
            a5[i] = {bf_lo(n), bf_hi(n)};
        }
        char* rb = smem + wv * (ROWBUF * 8);
        fft5<-1>(a5);
        lds_st(rb, lane, a5[0]);
#pragma unroll
        for (int p = 1; p < 5; ++p) lds_st(rb, p * 72 + lane, cmul(a5[p], tw5[p]));
        wave_fence();
        cf a8[8];
        if (lane < 40) {
#pragma unroll
            for (int t2 = 0; t2 < 8; ++t2) a8[t2] = lds_ld(rb, pp * 72 + t1 + 8 * t2);
            fft8<-1>(a8);
            wave_fence();
            lds_st(rb, pp * 72 + t1, a8[0]);
#pragma unroll
            for (int a = 1; a < 8; ++a) lds_st(rb, pp * 72 + 9 * a + t1, cmul(a8[a], tw8[a]));
            wave_fence();
#pragma unroll
            for (int t = 0; t < 8; ++t) a8[t] = lds_ld(rb, pp * 72 + 9 * t1 + t);
            fft8<-1>(a8);
            wave_fence();
#pragma unroll
            for (int bb = 0; bb < 8; ++bb) lds_st(rb, pp + 5 * t1 + 40 * bb, a8[bb]);
        }
        __syncthreads();
        if (tid < DF_) {
            const int k = tid;
            const int ka = k == 320 ? 0 : k, kb = k == 0 ? 0 : 320 - k;
#pragma unroll
            for (int j = 0; j < 8; ++j) {
                cf A = lds_ld(smem + j * (ROWBUF * 8), ka), B = lds_ld(smem + j * (ROWBUF * 8), kb);
                cf E = {A.x + B.x, A.y - B.y}, O = {A.x - B.x, A.y + B.y};
                a8[j] = E + cmul(O, twk);
            }
            fft8<-1>(a8);
        }
        cf tq[8];
#pragma unroll
        for (int q = 1; q < 8; ++q) tq[q] = {rdlane(twq.x, q), rdlane(twq.y, q)};
        if (tid < DF_) {
            const int k = tid;
            f32x2* T = (f32x2*)(ws + WS_T) + ((k >> 2) * 256 + s0) * 4 + (k & 3);
            T[0] = f32x2{a8[0].x, a8[0].y};
#pragma unroll
            for (int q = 1; q < 8; ++q) {
                cf r = cmul(a8[q], tq[q]);
                T[q * (NG4 * 1024)] = f32x2{r.x, r.y};
            }
        }
    } else if (b < 256 + K1_WT_BLOCKS) {
        const int id = b - 256, q = id & 7, a = id >> 3;
        const int k = tid;
        if (k < 4 * NG4) {
            unsigned o[16];
            if (k < DF_) {
#pragma unroll
                for (int i = 0; i < 16; ++i) {
                    const int u = 8 * (a + 16 * i) + q;
                    o[i] = (unsigned)wr[u * DF_ + k] | ((unsigned)wi[u * DF_ + k] << 16);
                }
            } else {
#pragma unroll
                for (int i = 0; i < 16; ++i) o[i] = 0u;
            }
            u32x4* Wt = (u32x4*)(ws + WS_WT) + (q * NG4 + (k >> 2)) * 256 + a * 4 + (k & 3);
#pragma unroll
            for (int bq = 0; bq < 4; ++bq) Wt[bq * 64] = u32x4{o[4 * bq], o[4 * bq + 1], o[4 * bq + 2], o[4 * bq + 3]};
        }
    } else {
        const int id = b - 256 - K1_WT_BLOCKS;
#pragma unroll
        for (int r = 0; r < 2; ++r) {
            const int fr = id * 16 + wv * 2 + r;
            const int kb = fr / 40, nb = fr - kb * 40;
            const int n = 16 * nb + (lane & 15), g = lane >> 4;
            unsigned short e[8];
#pragma unroll
            for (int i = 0; i < 8; ++i) e[i] = w2[(32 * kb + 16 * (i >> 2) + 4 * g + (i & 3)) * D_ + n];
            u32x4 o = {(unsigned)e[0] | ((unsigned)e[1] << 16), (unsigned)e[2] | ((unsigned)e[3] << 16),
                       (unsigned)e[4] | ((unsigned)e[5] << 16), (unsigned)e[6] | ((unsigned)e[7] << 16)};
            const int nt2 = nb / 5, j = nb - nt2 * 5;
            ((u32x4*)(ws + WS_W2T))[((nt2 * 48 + kb) * 5 + j) * 64 + lane] = o;
        }
    }
}

constexpr int NW = 6, NX = 3;
constexpr int WGRP = 3104, WSLOT = 4 * WGRP, XSLOT = 16384;
constexpr int L2_X = 0, L2_W = NX * XSLOT, L2_F = L2_W + NW * WSLOT;
constexpr int FSCR = 16 * 68 * 8;
constexpr int K2_LDS = L2_F + 3 * FSCR;
constexpr int KSTEPS = 20;

constexpr int dma_wait_count(int i) {
    int pos = 0, posW[KSTEPS] = {}, posX[KSTEPS / 2] = {};
    for (int st = 0; st < NW; ++st) {
        if ((st & 1) == 0 && st / 2 < NX) { pos += 4; posX[st / 2] = pos; }
        pos += 3; posW[st] = pos;
    }
    for (int t = 0; t < i; ++t) {
        if ((t & 1) && (t - 1) / 2 + NX < KSTEPS / 2) { pos += 4; posX[(t - 1) / 2 + NX] = pos; }
        if (t + NW < KSTEPS) { pos += 3; posW[t + NW] = pos; }
    }
    int need = posW[i + 1] > posX[(i + 1) / 2] ? posW[i + 1] : posX[(i + 1) / 2];
    return pos - need;
}

struct Dma {
    unsigned xo[4], wo[3];
    const char* xb; const char* wb;
    unsigned ldsx, ldsw;
};
template <int ST> DEV void dma_x(const Dma& d) {
    const char* g = d.xb + ST * 128;
    const unsigned l = d.ldsx + (ST % NX) * XSLOT;
    unsigned keep;
    asm volatile("s_mov_b32 %0, m0\n\ts_mov_b32 m0, %6\n\ts_nop 0\n\t"
                 "global_load_lds_dwordx4 %1, %5\n\t"
                 "global_load_lds_dwordx4 %2, %5 offset:1024\n\t"
                 "global_load_lds_dwordx4 %3, %5 offset:2048\n\t"
                 "global_load_lds_dwordx4 %4, %5 offset:3072\n\t"
                 "s_mov_b32 m0, %0"
                 : "=&s"(keep) : "v"(d.xo[0]), "v"(d.xo[1]), "v"(d.xo[2]), "v"(d.xo[3]), "s"(g), "s"(l) : "memory");
}
template <int ST> DEV void dma_w(const Dma& d) {
    const char* g = d.wb + (size_t)ST * (32 * F_ * 2);
    const unsigned l = d.ldsw + (ST % NW) * WSLOT;
    unsigned keep;
    asm volatile("s_mov_b32 %0, m0\n\ts_mov_b32 m0, %5\n\ts_nop 0\n\t"
                 "global_load_lds_dwordx4 %1, %4\n\t"
                 "global_load_lds_dwordx4 %2, %4 offset:1024\n\t"
                 "global_load_lds_dwordx4 %3, %4 offset:2048\n\t"
                 "s_mov_b32 m0, %0"
                 : "=&s"(keep) : "v"(d.wo[0]), "v"(d.wo[1]), "v"(d.wo[2]), "s"(g), "s"(l) : "memory");
}
template <int N> DEV void wait_vm() { asm volatile("s_waitcnt vmcnt(%0)" ::"i"(N) : "memory"); }
template <int I> DEV void hsync(const Dma& d) {
    __builtin_amdgcn_sched_barrier(0);
    wait_vm<dma_wait_count(I)>();
    __builtin_amdgcn_s_barrier();
    if constexpr (I >= 0) {
        if constexpr ((I & 1) && (I - 1) / 2 + NX < KSTEPS / 2) dma_x<(I - 1) / 2 + NX>(d);
        if constexpr (I + NW < KSTEPS) dma_w<I + NW>(d);
    }
    __builtin_amdgcn_sched_barrier(0);
}

DEV float silu_f(float v) { return v * __builtin_amdgcn_rcpf(1.0f + __builtin_amdgcn_exp2f(v * -1.44269504088896341f)); }

struct Frags { s16x8 x[4]; s16x8 w[6]; };
DEV void k2_read(Frags& f, const char* smem, unsigned xa, unsigned wa) {
#pragma unroll
    for (int tb = 0; tb < 4; ++tb) f.x[tb] = *(const LAS s16x8*)((LAS const char*)smem + xa + tb * 2048);
#pragma unroll
    for (int i = 0; i < 6; ++i) {
        const int off = (i / 3) * (2 * WGRP) + (i % 3) * 32;
        s16x4 lo = __builtin_amdgcn_ds_read_tr16_b64_v4i16((LAS s16x4*)((LAS char*)smem + wa + off));
        s16x4 hi = __builtin_amdgcn_ds_read_tr16_b64_v4i16((LAS s16x4*)((LAS char*)smem + wa + off + 768));
        f.w[i] = __builtin_shufflevector(lo, hi, 0, 1, 2, 3, 4, 5, 6, 7);
    }
}
DEV void k2_mma(f32x4 (&acc)[6][4], const Frags& f) {
#pragma unroll
    for (int i = 0; i < 6; ++i)
#pragma unroll
        for (int tb = 0; tb < 4; ++tb)
            acc[i][tb] = __builtin_amdgcn_mfma_f32_16x16x32_bf16(__builtin_bit_cast(bf16x8, f.w[i]), __builtin_bit_cast(bf16x8, f.x[tb]), acc[i][tb], 0, 0, 0);
}

__global__ NOPK __launch_bounds__(512, 2) void k2_kernel(const char* __restrict__ w1, const char* __restrict__ w3, char* __restrict__ ws) {
    extern __shared__ __attribute__((aligned(16))) char smem[];
    const int b = blockIdx.x, tid = threadIdx.x, lane = tid & 63;
    const int wv = __builtin_amdgcn_readfirstlane(tid >> 6);
    const int xcd = b & 7, idx = b >> 3;
    const int mt = (xcd & 1) * 8 + (idx & 7), nt = (xcd >> 1) * 4 + (idx >> 3);
    if (wv < 4) {
        const int wm = wv & 1, wn = wv >> 1;
        const int g = lane >> 4, r16 = lane & 15;
        const int sg = ((g & 1) << 1) | (g >> 1);
        const unsigned xa0 = L2_X + (64 * wm + r16) * 128 + ((sg ^ (((r16 >> 1) + 2) & 7)) << 4);
        const unsigned wa0 = L2_W + (g & 1) * WGRP + (8 * (g >> 1) + (r16 >> 2)) * 192 + (48 * wn + 4 * (lane & 3)) * 2;
        f32x4 acc[6][4];
#pragma unroll
        for (int i = 0; i < 6; ++i)
#pragma unroll
            for (int tb = 0; tb < 4; ++tb) acc[i][tb] = f32x4{0.f, 0.f, 0.f, 0.f};
        Frags fa, fb;
        __builtin_amdgcn_s_barrier();
        asm volatile("" ::: "memory");
        k2_read(fa, smem, xa0, wa0);
        int ws_ = 1, xs_ = 0;
        for (int it = 0; it < KSTEPS / 2; ++it) {
            asm volatile("s_waitcnt lgkmcnt(0)" ::: "memory");
            __builtin_amdgcn_sched_barrier(0);
            __builtin_amdgcn_s_barrier();
            asm volatile("" ::: "memory");
            k2_read(fb, smem, (xa0 ^ 64) + xs_ * XSLOT, wa0 + ws_ * WSLOT);
            ws_ = ws_ + 1 == NW ? 0 : ws_ + 1;
            xs_ = xs_ + 1 == NX ? 0 : xs_ + 1;
            k2_mma(acc, fa);
            asm volatile("s_waitcnt lgkmcnt(0)" ::: "memory");
            __builtin_amdgcn_sched_barrier(0);
            if (it < KSTEPS / 2 - 1) {
                __builtin_amdgcn_s_barrier();
                asm volatile("" ::: "memory");
                k2_read(fa, smem, xa0 + xs_ * XSLOT, wa0 + ws_ * WSLOT);
                ws_ = ws_ + 1 == NW ? 0 : ws_ + 1;
            }
            k2_mma(acc, fb);
        }
        char* ht = ws + WS_HT;
        const int mt2 = mt * 2 + wm;
        const int FB0 = 6 * nt + 3 * wn;
#pragma unroll
        for (int tb = 0; tb < 4; ++tb) {
            u32x2 hv[3];
#pragma unroll
            for (int i = 0; i < 3; ++i) {
                float h[4];
#pragma unroll
                for (int r = 0; r < 4; ++r) h[r] = rbf(silu_f(rbf(acc[i][tb][r]))) * rbf(acc[3 + i][tb][r]);
                hv[i] = u32x2{pack_bf16(h[0], h[1]), pack_bf16(h[2], h[3])};
            }
            if (wn == 0) {
                const int kb = FB0 >> 1;
                *(u32x4*)(ht + ((size_t)((mt2 * 48 + kb) * 4 + tb) * 64 + lane) * 16) = u32x4{hv[0].x, hv[0].y, hv[1].x, hv[1].y};
                *(u32x2*)(ht + ((size_t)((mt2 * 48 + kb + 1) * 4 + tb) * 64 + lane) * 16) = hv[2];
            } else {
                const int kb = FB0 >> 1;
                *(u32x2*)(ht + ((size_t)((mt2 * 48 + kb) * 4 + tb) * 64 + lane) * 16 + 8) = hv[0];
                *(u32x4*)(ht + ((size_t)((mt2 * 48 + kb + 1) * 4 + tb) * 64 + lane) * 16) = u32x4{hv[1].x, hv[1].y, hv[2].x, hv[2].y};
            }
        }
    } else {
        const int hw = wv - 4;
        const int U = hw * 256 + b;
        const bool has = U < NUNIT;
        f32x2* Tu = (f32x2*)(ws + WS_T) + (size_t)U * 1024 + lane;
        cf v[16];
        u32x4 wq[4];
        if (has) {
#pragma unroll
            for (int i = 0; i < 16; ++i) { f32x2 t = Tu[64 * i]; v[i] = {t.x, t.y}; }
#pragma unroll
            for (int i = 0; i < 4; ++i) wq[i] = ((const u32x4*)(ws + WS_WT))[(size_t)U * 256 + i * 64 + lane];
        }
        Dma d;
        const unsigned lbase = (unsigned)(uintptr_t)(LAS char*)smem;
#pragma unroll
        for (int r = 0; r < 4; ++r) {
            const int row = 8 * (4 * hw + r) + (lane >> 3);
            const int c = (lane & 7) ^ (((row >> 1) + 2) & 7);
            d.xo[r] = (unsigned)(row * (D_ * 2) + c * 16 - r * 1024);
        }
#pragma unroll
        for (int r = 0; r < 3; ++r) {
            const int L = r * 64 + lane, row = L / 12, c = L - row * 12;
            d.wo[r] = (unsigned)((16 * (hw & 1) + row) * (F_ * 2) + c * 16 - r * 1024);
        }
        d.xb = ws + WS_XN + (size_t)mt * 128 * (D_ * 2);
        d.wb = (hw >> 1 ? w3 : w1) + nt * 192;
        d.ldsx = lbase + L2_X + hw * 4096;
        d.ldsw = lbase + L2_W + hw * WGRP;
        dma_x<0>(d); dma_w<0>(d); dma_w<1>(d);
        dma_x<1>(d); dma_w<2>(d); dma_w<3>(d);
        dma_x<2>(d); dma_w<4>(d); dma_w<5>(d);
        const int t = lane >> 2;
        cf tw[16];
        if (has) {
#pragma unroll
            for (int a = 1; a < 16; ++a) tw[a] = twid((float)(t * a) * (1.0f / 256.0f));
        }
        char* fs = smem + L2_F + hw * FSCR;
        hsync<-1>(d);
        hsync<0>(d);
        if (has) fft16_a<-1>(v);
        hsync<1>(d);
        if (has) fft16_b<-1>(v);
        hsync<2>(d);
        if (has) {
            lds_st(fs, lane, v[0]);
#pragma unroll
            for (int a = 1; a < 16; ++a) lds_st(fs, a * 68 + lane, cmul(v[a], tw[a]));
            wave_fence();
#pragma unroll
            for (int i = 0; i < 16; ++i) v[i] = lds_ld(fs, t * 68 + i * 4 + (lane & 3));
        }
        hsync<3>(d);
        if (has) fft16_a<-1>(v);
        hsync<4>(d);
        if (has) fft16_b<-1>(v);
        hsync<5>(d);
        if (has) {
#pragma unroll
            for (int i = 0; i < 16; ++i) { const unsigned w = wq[i >> 2][i & 3]; v[i] = cmul(v[i], cf{bf_lo(w), bf_hi(w)}); }
        }
        hsync<6>(d);
        if (has) fft16_a<1>(v);
        hsync<7>(d);
        if (has) fft16_b<1>(v);
        hsync<8>(d);
        if (has) {
            wave_fence();
            lds_st(fs, t * 68 + (lane & 3), v[0]);
#pragma unroll
            for (int i = 1; i < 16; ++i) lds_st(fs, t * 68 + i * 4 + (lane & 3), cmulc(v[i], tw[i]));
            wave_fence();
#pragma unroll
            for (int a = 0; a < 16; ++a) v[a] = lds_ld(fs, a * 68 + lane);
        }
        hsync<9>(d);
        if (has) fft16_a<1>(v);
        hsync<10>(d);
        if (has) fft16_b<1>(v);
        hsync<11>(d);
        hsync<12>(d);
        hsync<13>(d);
        hsync<14>(d);
        hsync<15>(d);
        hsync<16>(d);
        hsync<17>(d);
        hsync<18>(d);
        if (has) {
#pragma unroll
            for (int i = 0; i < 16; ++i) Tu[64 * i] = f32x2{v[i].x, v[i].y};
        }
    }
}

constexpr int L3_RED = 0;
constexpr int L3_ROW = 61440;
constexpr int L3_S = L3_ROW + 8 * ROWBUF * 8;
constexpr int SPITCH = 176;
constexpr int L3_CNT = L3_S + 64 * SPITCH;
constexpr int K3_LDS = L3_CNT + 64;
constexpr int PD = 4;

DEV void helper_barrier(char* smem, int lane, unsigned target) {
    asm volatile("s_waitcnt lgkmcnt(0)" ::: "memory");
    LAS unsigned* cnt = (LAS unsigned*)((LAS char*)smem + L3_CNT);
    if (lane == 0) __hip_atomic_fetch_add(cnt, 1u, __ATOMIC_RELAXED, __HIP_MEMORY_SCOPE_WORKGROUP);
    while (__hip_atomic_load(cnt, __ATOMIC_RELAXED, __HIP_MEMORY_SCOPE_WORKGROUP) < target) __builtin_amdgcn_s_sleep(1);
    asm volatile("" ::: "memory");
}

__global__ NOPK __launch_bounds__(512, 2) void k3_kernel(const char* __restrict__ x, char* __restrict__ ws, char* __restrict__ out) {
    extern __shared__ __attribute__((aligned(16))) char smem[];
    const int b = blockIdx.x, tid = threadIdx.x, lane = tid & 63;
    const int wv = __builtin_amdgcn_readfirstlane(tid >> 6);
    const int xcd = b & 7, idx = b >> 3;
    const int mt2 = (xcd & 3) * 8 + (idx & 7), nt2 = (xcd >> 2) * 4 + (idx >> 3);
    if (wv < 4) {
        const u32x4* hp = (const u32x4*)(ws + WS_HT) + (size_t)(mt2 * 48 + 12 * wv) * 256 + lane;
        const u32x4* wp = (const u32x4*)(ws + WS_W2T) + (size_t)(nt2 * 48 + 12 * wv) * 320 + lane;
        u32x4 hf[PD][4], wf[PD][5];
#pragma unroll
        for (int st = 0; st < PD; ++st) {
#pragma unroll
            for (int j = 0; j < 5; ++j) wf[st][j] = wp[st * 320 + j * 64];
#pragma unroll
            for (int tb = 0; tb < 4; ++tb) hf[st][tb] = hp[st * 256 + tb * 64];
        }
        __builtin_amdgcn_s_barrier();
        f32x4 acc[5][4];
#pragma unroll
        for (int j = 0; j < 5; ++j)
#pragma unroll
            for (int tb = 0; tb < 4; ++tb) acc[j][tb] = f32x4{0.f, 0.f, 0.f, 0.f};
#pragma unroll
        for (int st = 0; st < 12; ++st) {
            const int sl = st % PD;
#pragma unroll
            for (int j = 0; j < 5; ++j)
#pragma unroll
                for (int tb = 0; tb < 4; ++tb)
                    acc[j][tb] = __builtin_amdgcn_mfma_f32_16x16x32_bf16(__builtin_bit_cast(bf16x8, wf[sl][j]), __builtin_bit_cast(bf16x8, hf[sl][tb]), acc[j][tb], 0, 0, 0);
            if (st + PD < 12) {
#pragma unroll
                for (int j = 0; j < 5; ++j) wf[sl][j] = wp[(st + PD) * 320 + j * 64];
#pragma unroll
                for (int tb = 0; tb < 4; ++tb) hf[sl][tb] = hp[(st + PD) * 256 + tb * 64];
            }
        }
        const int tok = lane & 15, g = lane >> 4;
        const size_t rowoff = (size_t)(mt2 * 64 + 16 * wv + tok) * (D_ * 2) + (nt2 * 80 + 4 * g) * 2;
        u32x2 xv[5];
#pragma unroll
        for (int j = 0; j < 5; ++j) xv[j] = *(const u32x2*)(x + rowoff + j * 32);
#pragma unroll
        for (int tb = 0; tb < 4; ++tb) {
            if (tb != wv) {
                const int slot = tb * 3 + (wv > tb ? wv - 1 : wv);
#pragma unroll
                for (int j = 0; j < 5; ++j) *(LAS f32x4*)((LAS char*)smem + L3_RED + (slot * 5 + j) * 1024 + lane * 16) = acc[j][tb];
            }
        }
        f32x4 o[5];
#pragma unroll
        for (int j = 0; j < 5; ++j) o[j] = wv == 0 ? acc[j][0] : wv == 1 ? acc[j][1] : wv == 2 ? acc[j][2] : acc[j][3];
        __syncthreads();
#pragma unroll
        for (int p = 0; p < 3; ++p)
#pragma unroll
            for (int j = 0; j < 5; ++j) o[j] += *(const LAS f32x4*)((LAS const char*)smem + L3_RED + ((wv * 3 + p) * 5 + j) * 1024 + lane * 16);
#pragma unroll
        for (int j = 0; j < 5; ++j) {
            const u32x2 sv = *(const LAS u32x2*)((LAS const char*)smem + L3_S + (16 * wv + tok) * SPITCH + j * 32 + g * 8);
            float gq[4];
#pragma unroll
            for (int r = 0; r < 4; ++r) gq[r] = rbf(silu_f(rbf(o[j][r])));
            const unsigned p0 = pack_bf16(bf_lo(sv.x) * gq[0], bf_hi(sv.x) * gq[1]);
            const unsigned p1 = pack_bf16(bf_lo(sv.y) * gq[2], bf_hi(sv.y) * gq[3]);
            u32x2 r2 = {pack_bf16(bf_lo(xv[j].x) + bf_lo(p0), bf_hi(xv[j].x) + bf_hi(p0)),
                        pack_bf16(bf_lo(xv[j].y) + bf_lo(p1), bf_hi(xv[j].y) + bf_hi(p1))};
            *(u32x2*)(out + rowoff + j * 32) = r2;
        }
    } else {
        const int hw = wv - 4, ht = tid - 256;
        const int s0 = (b & 7) * 32 + (b >> 3);
        const int k = ht <= 160 ? ht : 160, k2 = 320 - k;
        const f32x2* V = (const f32x2*)(ws + WS_T);
        cf A[8], B[8];
#pragma unroll
        for (int q = 0; q < 8; ++q) {
            f32x2 ta = V[(size_t)q * (NG4 * 1024) + ((k >> 2) * 256 + s0) * 4 + (k & 3)];
            f32x2 tb = V[(size_t)q * (NG4 * 1024) + ((k2 >> 2) * 256 + s0) * 4 + (k2 & 3)];
            A[q] = {ta.x, ta.y}; B[q] = {tb.x, tb.y};
        }
        if (tid == 256) *(LAS unsigned*)((LAS char*)smem + L3_CNT) = 0u;
        asm volatile("s_waitcnt lgkmcnt(0)" ::: "memory");
        __builtin_amdgcn_s_barrier();
        constexpr float NORM = 1.0f / (2.0f * 2048.0f * 640.0f);
        cf twq = twid((float)(s0 * (lane & 7)) * (1.0f / 2048.0f));
        twq.x *= NORM; twq.y *= NORM;
        cf twk = twid((float)k * (1.0f / 640.0f));
        twk = cf{twk.y, twk.x};
        cf tw5[5], tw8[8];
        const int pp = lane >> 3, t1 = lane & 7;
#pragma unroll
        for (int p = 1; p < 5; ++p) tw5[p] = twid((float)(lane * p) * (1.0f / 320.0f));
#pragma unroll
        for (int a = 1; a < 8; ++a) tw8[a] = twid((float)(t1 * a) * (1.0f / 64.0f));
#pragma unroll
        for (int q = 0; q < 8; ++q) {
            const cf w = {rdlane(twq.x, q), rdlane(twq.y, q)};
            A[q] = cmulc(A[q], w); B[q] = cmulc(B[q], w);
        }
        fft8<1>(A); fft8<1>(B);
        const float keepim = k == 0 ? 0.f : 1.f;
        const int kw2 = k == 0 ? 0 : k2;
        char* rows = smem + L3_ROW;
#pragma unroll
        for (int j = 0; j < 8; ++j) {
            cf a = A[j], bb = B[j];
            if (k == 0) { a.y = 0.f; bb.y = 0.f; }
            cf Sx = {a.x + bb.x, a.y - bb.y}, Dx = {a.x - bb.x, a.y + bb.y};
            cf P = cmul(Dx, twk);
            lds_st(rows + j * (ROWBUF * 8), k, Sx + P);
            lds_st(rows + j * (ROWBUF * 8), kw2, cf{Sx.x - P.x, P.y - Sx.y});
        }
        (void)keepim;
        helper_barrier(smem, lane, 4);
        char* sg = ws + WS_S;
        for (int rr = 0; rr < 2; ++rr) {
            const int j = 2 * hw + rr;
            char* rb = rows + j * (ROWBUF * 8);
            cf a8[8];
            if (lane < 40) {
#pragma unroll
                for (int bb = 0; bb < 8; ++bb) a8[bb] = lds_ld(rb, pp + 5 * t1 + 40 * bb);
                fft8<1>(a8);
                wave_fence();
                lds_st(rb, pp * 72 + 9 * t1, a8[0]);
#pragma unroll
                for (int t = 1; t < 8; ++t) lds_st(rb, pp * 72 + 9 * t1 + t, cmulc(a8[t], tw8[t]));
                wave_fence();
#pragma unroll
                for (int a = 0; a < 8; ++a) a8[a] = lds_ld(rb, pp * 72 + 9 * a + t1);
                fft8<1>(a8);
                wave_fence();
#pragma unroll
                for (int t2 = 0; t2 < 8; ++t2) lds_st(rb, pp * 72 + t1 + 8 * t2, a8[t2]);
            }
            wave_fence();
            cf a5[5];
            a5[0] = lds_ld(rb, lane);
#pragma unroll
            for (int p = 1; p < 5; ++p) a5[p] = cmulc(lds_ld(rb, p * 72 + lane), tw5[p]);
            fft5<1>(a5);
            wave_fence();
#pragma unroll
            for (int i = 0; i < 5; ++i) *(LAS unsigned*)((LAS char*)rb + (lane + 64 * i) * 4) = pack_bf16(a5[i].x, a5[i].y);
            wave_fence();
            char* dst = sg + (size_t)(s0 + 256 * j) * (D_ * 2);
            {
                u32x4 o = *(const LAS u32x4*)((LAS const char*)rb + lane * 16);
                asm volatile("global_store_dwordx4 %0, %1, off sc1\n\ts_nop 1" ::"v"(dst + lane * 16), "v"(o) : "memory");
            }
            if (lane < 16) {
                u32x4 o = *(const LAS u32x4*)((LAS const char*)rb + 1024 + lane * 16);
                asm volatile("global_store_dwordx4 %0, %1, off sc1\n\ts_nop 1" ::"v"(dst + 1024 + lane * 16), "v"(o) : "memory");
            }
        }
        asm volatile("s_waitcnt vmcnt(0)" ::: "memory");
        helper_barrier(smem, lane, 8);
        unsigned* flags = (unsigned*)(ws + WS_FLAG);
        if (tid == 256) __hip_atomic_store(flags + s0, 1u, __ATOMIC_RELAXED, __HIP_MEMORY_SCOPE_AGENT);
        const int f0 = (mt2 * 64) & 255;
        for (unsigned spin = 0; spin < (1u << 22); ++spin) {
            const unsigned f = __hip_atomic_load(flags + f0 + lane, __ATOMIC_RELAXED, __HIP_MEMORY_SCOPE_AGENT);
            if (__all(f == 1u)) break;
            __builtin_amdgcn_s_sleep(2);
        }
        asm volatile("" ::: "memory");
#pragma unroll
        for (int r = 0; r < 3; ++r) {
            const int pc = r * 256 + ht;
            if (pc < 640) {
                const int row = pc / 10, c = pc - row * 10;
                const char* src = sg + (size_t)(mt2 * 64 + row) * (D_ * 2) + nt2 * 160 + c * 16;
                u32x4 o;
                asm volatile("global_load_dwordx4 %0, %1, off sc1\n\ts_waitcnt vmcnt(0)" : "=&v"(o) : "v"(src) : "memory");
                *(LAS u32x4*)((LAS char*)smem + L3_S + row * SPITCH + c * 16) = o;
            }
        }
        __syncthreads();
    }
}

extern "C" void kernel_launch(void* const* d_in, const int* in_sizes, int n_in, void* d_out, int out_size, void* d_ws,
                              size_t ws_size, hipStream_t stream) {
    (void)in_sizes; (void)n_in; (void)out_size; (void)ws_size;
    char* ws = (char*)d_ws;
    hipFuncSetAttribute((const void*)k2_kernel, hipFuncAttributeMaxDynamicSharedMemorySize, K2_LDS);
    hipFuncSetAttribute((const void*)k3_kernel, hipFuncAttributeMaxDynamicSharedMemorySize, K3_LDS);
    k1_kernel<<<256 + K1_WT_BLOCKS + K1_W2_BLOCKS, 512, 0, stream>>>((const unsigned*)d_in[0], (const unsigned short*)d_in[1],
                                                                     (const unsigned short*)d_in[2], (const unsigned short*)d_in[6], ws);
    k2_kernel<<<256, 512, K2_LDS, stream>>>((const char*)d_in[4], (const char*)d_in[5], ws);
    k3_kernel<<<256, 512, K3_LDS, stream>>>((const char*)d_in[0], ws, (char*)d_out);
}
```

```cpp
#include <hip/hip_runtime.h>
#include <hip/hip_bf16.h>
#include <stdint.h>

typedef float f32x2 __attribute__((ext_vector_type(2)));
typedef float f32x4 __attribute__((ext_vector_type(4)));
typedef float f32x16 __attribute__((ext_vector_type(16)));
typedef short s16x4 __attribute__((ext_vector_type(4)));
typedef short s16x8 __attribute__((ext_vector_type(8)));
typedef __bf16 bf16x2 __attribute__((ext_vector_type(2)));
typedef __bf16 bf16x8 __attribute__((ext_vector_type(8)));
typedef _Float16 f16x2 __attribute__((ext_vector_type(2)));
typedef unsigned u32x2 __attribute__((ext_vector_type(2)));
typedef unsigned u32x4 __attribute__((ext_vector_type(4)));

#define DEV __device__ __forceinline__
#define LAS __attribute__((address_space(3)))
#if defined(__HIP_DEVICE_COMPILE__)
#define NOPK __attribute__((target("no-packed-fp32-ops")))
#else
#define NOPK
#endif

constexpr int S_ = 2048, D_ = 640, F_ = 1536, DF_ = 321;
constexpr int NG4 = 81;
constexpr int NUNIT = 8 * NG4;

constexpr size_t WS_XN = 0;
constexpr size_t WS_T = 4u << 20;
constexpr size_t WS_WT = 12u << 20;
constexpr size_t WS_W2T = 16u << 20;
constexpr size_t WS_HT = 20u << 20;
constexpr size_t WS_S = 28u << 20;
constexpr size_t WS_T16 = 32u << 20;
constexpr size_t WS_HINT = 36u << 20;
constexpr int TP = 328;

constexpr size_t WS_DBG = 64u << 20;
#define STAMP_DECL
#define STAMP(kern, id) do {} while (0)
#define CSTAMP(kern, id) do {} while (0)
#define STAMP_FLUSH(kern) do {} while (0)

struct cf { float x, y; };
DEV cf operator+(cf a, cf b) { return {a.x + b.x, a.y + b.y}; }
DEV cf operator-(cf a, cf b) { return {a.x - b.x, a.y - b.y}; }
DEV cf cmul(cf a, cf w) { return {a.x * w.x - a.y * w.y, a.x * w.y + a.y * w.x}; }
DEV cf cmulc(cf a, cf w) { return {a.x * w.x + a.y * w.y, a.y * w.x - a.x * w.y}; }
template <int DIR> DEV cf muli(cf a) { return DIR > 0 ? cf{-a.y, a.x} : cf{a.y, -a.x}; }
DEV cf twid(float f) { return {__builtin_amdgcn_cosf(f), -__builtin_amdgcn_sinf(f)}; }

DEV unsigned pack_bf16(float a, float b) {
    f32x2 v = {a, b};
    return __builtin_bit_cast(unsigned, __builtin_convertvector(v, bf16x2));
}
DEV float bf_lo(unsigned u) { return __uint_as_float(u << 16); }
DEV float bf_hi(unsigned u) { return __uint_as_float(u & 0xffff0000u); }
DEV float rbf(float a) { return bf_lo(pack_bf16(a, a)); }

template <int DIR> DEV void fft4(cf& a0, cf& a1, cf& a2, cf& a3) {
    cf t0 = a0 + a2, t1 = a0 - a2, t2 = a1 + a3, t3 = muli<DIR>(a1 - a3);
    a0 = t0 + t2; a2 = t0 - t2; a1 = t1 + t3; a3 = t1 - t3;
}
constexpr float RH = 0.70710678118654752f;
template <int DIR> DEV cf mulw8(cf a) { return DIR > 0 ? cf{(a.x - a.y) * RH, (a.x + a.y) * RH} : cf{(a.x + a.y) * RH, (a.y - a.x) * RH}; }
template <int DIR> DEV cf mulw83(cf a) { return DIR > 0 ? cf{-(a.x + a.y) * RH, (a.x - a.y) * RH} : cf{(a.y - a.x) * RH, -(a.x + a.y) * RH}; }

template <int DIR> DEV void fft8(cf (&a)[8]) {
    fft4<DIR>(a[0], a[2], a[4], a[6]);
    fft4<DIR>(a[1], a[3], a[5], a[7]);
    cf o1 = mulw8<DIR>(a[3]), o2 = muli<DIR>(a[5]), o3 = mulw83<DIR>(a[7]);
    cf e0 = a[0], e1 = a[2], e2 = a[4], e3 = a[6], o0 = a[1];
    a[0] = e0 + o0; a[4] = e0 - o0;
    a[1] = e1 + o1; a[5] = e1 - o1;
    a[2] = e2 + o2; a[6] = e2 - o2;
    a[3] = e3 + o3; a[7] = e3 - o3;
}
template <int DIR> DEV void fft5(cf (&a)[5]) {
    constexpr float c1 = 0.30901699437494742f, c2 = -0.80901699437494742f;
    constexpr float s1 = 0.95105651629515357f, s2 = 0.58778525229247313f;
    cf t1 = a[1] + a[4], t2 = a[2] + a[3], t3 = a[1] - a[4], t4 = a[2] - a[3];
    cf m1 = {a[0].x + c1 * t1.x + c2 * t2.x, a[0].y + c1 * t1.y + c2 * t2.y};
    cf m2 = {a[0].x + c2 * t1.x + c1 * t2.x, a[0].y + c2 * t1.y + c1 * t2.y};
    cf n1 = muli<DIR>(cf{s1 * t3.x + s2 * t4.x, s1 * t3.y + s2 * t4.y});
    cf n2 = muli<DIR>(cf{s2 * t3.x - s1 * t4.x, s2 * t3.y - s1 * t4.y});
    a[0] = a[0] + t1 + t2;
    a[1] = m1 + n1; a[4] = m1 - n1;
    a[2] = m2 + n2; a[3] = m2 - n2;
}
template <int DIR> DEV void fft10(cf (&a)[10]) {
    cf e[5], o[5];
#pragma unroll
    for (int n2 = 0; n2 < 5; ++n2) { const cf p = a[(2 * n2) % 10], q = a[(2 * n2 + 5) % 10]; e[n2] = p + q; o[n2] = p - q; }
    fft5<DIR>(e); fft5<DIR>(o);
#pragma unroll
    for (int k2 = 0; k2 < 5; ++k2) { a[(6 * k2) % 10] = e[k2]; a[(6 * k2 + 5) % 10] = o[k2]; }
}
template <int DIR> DEV void fft16_a(cf (&a)[16]) {
#pragma unroll
    for (int n2 = 0; n2 < 4; ++n2) fft4<DIR>(a[n2], a[n2 + 4], a[n2 + 8], a[n2 + 12]);
}
template <int DIR> DEV void fft16_t(cf (&a)[16]) {
    constexpr float C1 = 0.92387953251128674f, S1 = 0.38268343236508977f;
    const cf w1 = {C1, DIR * S1}, w3 = {S1, DIR * C1}, w9 = {-C1, -DIR * S1};
    a[1 + 4] = cmul(a[1 + 4], w1);
    a[1 + 8] = mulw8<DIR>(a[1 + 8]);
    a[1 + 12] = cmul(a[1 + 12], w3);
    a[2 + 4] = mulw8<DIR>(a[2 + 4]);
    a[2 + 8] = muli<DIR>(a[2 + 8]);
    a[2 + 12] = mulw83<DIR>(a[2 + 12]);
    a[3 + 4] = cmul(a[3 + 4], w3);
    a[3 + 8] = mulw83<DIR>(a[3 + 8]);
    a[3 + 12] = cmul(a[3 + 12], w9);
}
template <int DIR> DEV void fft16_b(cf (&a)[16]) {
#pragma unroll
    for (int k1 = 0; k1 < 4; ++k1) fft4<DIR>(a[4 * k1], a[4 * k1 + 1], a[4 * k1 + 2], a[4 * k1 + 3]);
    cf b[16];
#pragma unroll
    for (int k1 = 0; k1 < 4; ++k1)
#pragma unroll
        for (int k2 = 0; k2 < 4; ++k2) b[k1 + 4 * k2] = a[4 * k1 + k2];
#pragma unroll
    for (int i = 0; i < 16; ++i) a[i] = b[i];
}

template <int CTRL, int RM> DEV float dpp_add(float v) {
    int t = __builtin_amdgcn_update_dpp(0, __float_as_int(v), CTRL, RM, 0xf, true);
    return v + __int_as_float(t);
}
DEV float wave_sum(float v) {
    v = dpp_add<0x111, 0xf>(v);
    v = dpp_add<0x112, 0xf>(v);
    v = dpp_add<0x114, 0xf>(v);
    v = dpp_add<0x118, 0xf>(v);
    v = dpp_add<0x142, 0xa>(v);
    v = dpp_add<0x143, 0xc>(v);
    return __int_as_float(__builtin_amdgcn_readlane(__float_as_int(v), 63));
}
DEV float rdlane(float v, int l) { return __int_as_float(__builtin_amdgcn_readlane(__float_as_int(v), l)); }
#define PIN(x) asm volatile("" : "+v"(x))
#define SB() __builtin_amdgcn_sched_barrier(0)
DEV void bar() { SB(); __builtin_amdgcn_s_barrier(); asm volatile("" ::: "memory"); SB(); }
DEV void wait_lgkm0() { asm volatile("s_waitcnt lgkmcnt(0)" ::: "memory"); }
DEV void st16_wt(void* p, u32x4 v) { asm volatile("global_store_dwordx4 %0, %1, off sc1\n\ts_nop 1" ::"v"(p), "v"(v) : "memory"); }
DEV void st8_wt(void* p, f32x2 v) { asm volatile("global_store_dwordx2 %0, %1, off sc1" ::"v"(p), "v"(v) : "memory"); }
DEV void wave_fence() { asm volatile("" ::: "memory"); __builtin_amdgcn_wave_barrier(); }

DEV cf lds_ld(const char* base, int idx) { f32x2 v = *(const LAS f32x2*)((LAS const char*)base + idx * 8); return {v.x, v.y}; }
DEV void lds_st(char* base, int idx, cf a) { f32x2 v = {a.x, a.y}; *(LAS f32x2*)((LAS char*)base + idx * 8) = v; }

typedef f32x2 c2;
DEV c2 addi(c2 a, c2 b) { c2 r; asm("v_pk_add_f32 %0, %1, %2 op_sel:[0,1] op_sel_hi:[1,0] neg_lo:[0,1]" : "=v"(r) : "v"(a), "v"(b)); return r; }
DEV c2 subi(c2 a, c2 b) { c2 r; asm("v_pk_add_f32 %0, %1, %2 op_sel:[0,1] op_sel_hi:[1,0] neg_hi:[0,1]" : "=v"(r) : "v"(a), "v"(b)); return r; }
DEV c2 addc(c2 a, c2 b) { c2 r; asm("v_pk_add_f32 %0, %1, %2 neg_hi:[0,1]" : "=v"(r) : "v"(a), "v"(b)); return r; }
DEV c2 subc(c2 a, c2 b) { c2 r; asm("v_pk_add_f32 %0, %1, %2 neg_lo:[0,1]" : "=v"(r) : "v"(a), "v"(b)); return r; }
DEV c2 pmul(c2 a, c2 w) {
    c2 t = a.xx * w, r;
    asm("v_pk_fma_f32 %0, %1, %2, %3 op_sel:[1,1,0] op_sel_hi:[1,0,1] neg_lo:[0,1,0]" : "=v"(r) : "v"(a), "v"(w), "v"(t));
    return r;
}
DEV c2 pmulc(c2 a, c2 w) {
    c2 t = a.xx * w, r;
    asm("v_pk_fma_f32 %0, %1, %2, %3 op_sel:[1,1,0] op_sel_hi:[1,0,1] neg_hi:[0,1,0]" : "=v"(r) : "v"(a), "v"(w), "v"(t));
    return r;
}
DEV c2 pfma(c2 a, float k, c2 c) { return __builtin_elementwise_fma(a, c2{k, k}, c); }
DEV c2 ptw(float f) { c2 r = {__builtin_amdgcn_cosf(f), -__builtin_amdgcn_sinf(f)}; PIN(r); return r; }
DEV void pfft4(c2& a0, c2& a1, c2& a2, c2& a3) {
    c2 t0 = a0 + a2, t1 = a0 - a2, t2 = a1 + a3, t3 = a1 - a3;
    a0 = t0 + t2; a2 = t0 - t2; a1 = subi(t1, t3); a3 = addi(t1, t3);
}
DEV void pfft8(c2 (&a)[8]) {
    pfft4(a[0], a[2], a[4], a[6]);
    pfft4(a[1], a[3], a[5], a[7]);
    const c2 e0 = a[0], e1 = a[2], e2 = a[4], e3 = a[6], o0 = a[1], o2 = a[5];
    const c2 t1 = subi(a[3], a[3]), t3 = addi(a[7], a[7]);
    a[0] = e0 + o0; a[4] = e0 - o0;
    a[1] = pfma(t1, RH, e1); a[5] = pfma(t1, -RH, e1);
    a[2] = subi(e2, o2); a[6] = addi(e2, o2);
    a[3] = pfma(t3, -RH, e3); a[7] = pfma(t3, RH, e3);
}
DEV void pfft5(c2 (&a)[5]) {
    constexpr float c1 = 0.30901699437494742f, cc2 = -0.80901699437494742f;
    constexpr float s1 = 0.95105651629515357f, s2 = 0.58778525229247313f;
    const c2 t1 = a[1] + a[4], t2 = a[2] + a[3], t3 = a[1] - a[4], t4 = a[2] - a[3];
    const c2 m1 = pfma(t2, cc2, pfma(t1, c1, a[0])), m2 = pfma(t2, c1, pfma(t1, cc2, a[0]));
    const c2 n1 = pfma(t4, s2, t3 * s1), n2 = pfma(t4, -s1, t3 * s2);
    a[0] = a[0] + t1 + t2;
    a[1] = subi(m1, n1); a[4] = addi(m1, n1);
    a[2] = subi(m2, n2); a[3] = addi(m2, n2);
}
DEV void pfft10(c2 (&a)[10]) {
    c2 e[5], o[5];
#pragma unroll
    for (int n2 = 0; n2 < 5; ++n2) { const c2 p = a[(2 * n2) % 10], q = a[(2 * n2 + 5) % 10]; e[n2] = p + q; o[n2] = p - q; }
    pfft5(e); pfft5(o);
#pragma unroll
    for (int k2 = 0; k2 < 5; ++k2) { a[(6 * k2) % 10] = e[k2]; a[(6 * k2 + 5) % 10] = o[k2]; }
}
DEV c2 pld(const char* base, int idx) { return *(const LAS c2*)((LAS const char*)base + idx * 8); }
DEV void pst(char* base, int idx, c2 a) { *(LAS c2*)((LAS char*)base + idx * 8) = a; }
DEV unsigned pack_f16(c2 a) { return __builtin_bit_cast(unsigned, __builtin_convertvector(a, f16x2)); }

constexpr int K1_WT_BLOCKS = 128, K1_W2_BLOCKS = 48, K1_WARM_BLOCKS = 0;
constexpr unsigned CANARY = 0x7f817f81u;
constexpr int BUFA = 4 * 720 * 8, BUFB = 4 * 656 * 8;

__global__ __launch_bounds__(512, 2) void k1_kernel(const unsigned* __restrict__ x32, const unsigned short* __restrict__ wr,
                                                   const unsigned short* __restrict__ wi, const char* __restrict__ w1,
                                                   const char* __restrict__ w3, const char* __restrict__ w2, char* __restrict__ ws) {
    __shared__ __attribute__((aligned(16))) char smem[BUFA + BUFB];
    const int b = blockIdx.x, tid = threadIdx.x, lane = tid & 63;
    const int wv = __builtin_amdgcn_readfirstlane(tid >> 6);
    STAMP_DECL;
    STAMP(0, 0);
    if (b < 256) {
        const int s0 = (b & 7) * 32 + (b >> 3);
        const int s = s0 + 256 * wv;
        unsigned z[5];
#pragma unroll
        for (int i = 0; i < 5; ++i) z[i] = x32[s * 320 + lane + 64 * i];
        const int qr = tid / 80, r = tid - qr * 80;
        c2 t640[8], t80[8];
        if (wv < 5) {
#pragma unroll
            for (int a = 1; a < 8; ++a) t640[a] = ptw((float)(r * a) * (1.0f / 640.0f));
#pragma unroll
            for (int a = 1; a < 8; ++a) t80[a] = ptw((float)((r >> 3) * a) * (1.0f / 80.0f));
        }
        const c2 twq = ptw((float)(s0 * (lane & 7)) * (1.0f / 2048.0f));
        SB();
        float v[10];
#pragma unroll
        for (int i = 0; i < 5; ++i) { v[2 * i] = bf_lo(z[i]); v[2 * i + 1] = bf_hi(z[i]); }
        const float c0 = rdlane(v[0], 0);
        STAMP(0, 1);
        float s1 = 0.f, s2 = 0.f;
#pragma unroll
        for (int i = 0; i < 10; ++i) { float d = v[i] - c0; s1 += d; s2 += d * d; }
        s1 = wave_sum(s1); s2 = wave_sum(s2);
        const float md = s1 * (1.0f / 640.0f);
        float var = s2 * (1.0f / 640.0f) - md * md;
        var = var < 0.f ? 0.f : var;
        const float m = rbf(c0 + md);
        const float sd = rbf(__builtin_amdgcn_sqrtf(rbf(rbf(var) + 1e-5f)));
        const float rs = __builtin_amdgcn_rcpf(sd);
        char* bufA = smem;
        char* bufB = smem + BUFA;
#pragma unroll
        for (int i = 0; i < 5; ++i) {
            unsigned d = pack_bf16(v[2 * i] - m, v[2 * i + 1] - m);
            *(LAS unsigned*)((LAS char*)bufB + wv * 1280 + (lane + 64 * i) * 4) = pack_bf16(bf_lo(d) * rs, bf_hi(d) * rs);
        }
        wait_lgkm0();
        bar();
        STAMP(0, 2);
        if (wv >= 5) {
            u32x4 pc[4];
#pragma unroll
            for (int i = 0; i < 4; ++i) { const int p = tid - 320 + 192 * i; pc[i] = *(const LAS u32x4*)((LAS const char*)bufB + (p < 640 ? p : 0) * 16); }
            wait_lgkm0();
            bar();
#pragma unroll
            for (int i = 0; i < 4; ++i) {
                const int p = tid - 320 + 192 * i, j = p / 80, c = p - j * 80, sr = s0 + 256 * j;
                if (p < 640) *(u32x4*)(ws + WS_XN + (size_t)(sr >> 7) * (20 * 8192) + (c >> 1) * 4096 + ((sr >> 5) & 3) * 1024 + (c & 1) * 512 + (sr & 31) * 16) = pc[i];
            }
            bar(); bar();
            STAMP(0, 5); STAMP_FLUSH(0);
            return;
        }
        {
            c2 xr[8];
#pragma unroll
            for (int j = 0; j < 8; ++j) { const unsigned w = *(const LAS unsigned*)((LAS const char*)bufB + j * 1280 + tid * 4); xr[j] = c2{bf_lo(w), bf_hi(w)}; }
            const c2 a0 = xr[0] + xr[4], a1 = xr[0] - xr[4], b0 = xr[2] + xr[6], b1 = xr[2] - xr[6];
            const c2 e0 = xr[1] + xr[5], e1 = xr[1] - xr[5], d0 = xr[3] + xr[7], d1 = xr[3] - xr[7];
            const c2 p = a0 + b0, q = e0 + d0, u = e1 - d1, w = e1 + d1;
            const c2 y0 = p + q, y4 = p - q, y2r = a0 - b0, y2i = d0 - e0;
            const c2 y1r = pfma(u, RH, a1), y1i = pfma(w, -RH, -b1), y3r = pfma(u, -RH, a1), y3i = pfma(w, -RH, b1);
            *(LAS f32x4*)((LAS char*)bufA + (0 * 720 + 2 * tid) * 8) = f32x4{y0.x, y4.x, y0.y, y4.y};
            *(LAS f32x4*)((LAS char*)bufA + (1 * 720 + 2 * tid) * 8) = f32x4{y1r.x, y1i.x, y1r.y, y1i.y};
            *(LAS f32x4*)((LAS char*)bufA + (2 * 720 + 2 * tid) * 8) = f32x4{y2r.x, y2i.x, y2r.y, y2i.y};
            *(LAS f32x4*)((LAS char*)bufA + (3 * 720 + 2 * tid) * 8) = f32x4{y3r.x, y3i.x, y3r.y, y3i.y};
        }
        wait_lgkm0();
        bar();
        c2 a8[8];
        {
#pragma unroll
            for (int i = 0; i < 8; ++i) a8[i] = pld(bufA, qr * 720 + r + 80 * i);
            pfft8(a8);
            pst(bufB, qr * 656 + r, a8[0]);
#pragma unroll
            for (int a = 1; a < 8; ++a) pst(bufB, qr * 656 + a * 82 + r, pmul(a8[a], t640[a]));
        }
        wait_lgkm0();
        bar();
        STAMP(0, 3);
        {
            const int ib = qr * 656 + (r & 7) * 82 + (r >> 3), ob = qr * 720 + (r >> 3) * 72 + (r & 7);
#pragma unroll
            for (int i = 0; i < 8; ++i) a8[i] = pld(bufB, ib + 10 * i);
            pfft8(a8);
            pst(bufA, ob, a8[0]);
#pragma unroll
            for (int a = 1; a < 8; ++a) pst(bufA, ob + 8 * a, pmul(a8[a], t80[a]));
        }
        wait_lgkm0();
        bar();
        STAMP(0, 4);
        if (wv < 4) {
            c2 f[10];
#pragma unroll
            for (int t = 0; t < 10; ++t) f[t] = pld(bufA, wv * 720 + t * 72 + lane);
            pfft10(f);
            unsigned* T = (unsigned*)(ws + WS_T16) + (size_t)s0 * (8 * TP);
            if (wv == 0) {
#pragma unroll
                for (int bb = 5; bb < 10; ++bb) pst(bufB, lane + 64 * (bb - 5), f[bb]);
                if (lane == 0) pst(bufB, 320, f[0]);
                wave_fence();
                const c2 w4 = {rdlane(twq.y, 4) * 0.5f, rdlane(twq.x, 4) * -0.5f};
#pragma unroll
                for (int bb = 0; bb < 5; ++bb) {
                    const c2 h = pld(bufB, 320 - lane - 64 * bb);
                    T[lane + 64 * bb] = pack_f16(addc(f[bb], h) * 0.5f);
                    T[4 * TP + lane + 64 * bb] = pack_f16(pmul(subc(f[bb], h), w4));
                }
                if (lane == 0) {
                    T[320] = pack_f16(c2{f[5].x, 0.f});
                    T[4 * TP + 320] = pack_f16(pmul(subc(f[5], f[5]), w4));
                }
            } else {
                const c2 wa = {rdlane(twq.x, wv), rdlane(twq.y, wv)}, wb = {rdlane(twq.x, 8 - wv), rdlane(twq.y, 8 - wv)};
                unsigned* Ta = T + wv * TP;
                unsigned* Tb = T + (8 - wv) * TP;
#pragma unroll
                for (int bb = 0; bb < 5; ++bb) Ta[lane + 64 * bb] = pack_f16(pmul(f[bb], wa));
#pragma unroll
                for (int bb = 5; bb < 10; ++bb) Tb[640 - lane - 64 * bb] = pack_f16(pmulc(f[bb], wb));
                if (lane == 0) {
                    Tb[0] = pack_f16(pmulc(f[0], wb));
                    Ta[320] = pack_f16(pmul(f[5], wa));
                }
            }
        }
        STAMP(0, 5); STAMP_FLUSH(0);
    } else if (b < 256 + K1_WT_BLOCKS) {
        const int id = b - 256, q = id & 7, a = id >> 3;
        const int k = tid;
        if (k < 4 * NG4) {
            unsigned o[16];
            if (k < DF_) {
#pragma unroll
                for (int i = 0; i < 16; ++i) {
                    const int u = 8 * (a + 16 * i) + q;
                    o[i] = (unsigned)wr[u * DF_ + k] | ((unsigned)wi[u * DF_ + k] << 16);
                }
            } else {
#pragma unroll
                for (int i = 0; i < 16; ++i) o[i] = 0u;
            }
            u32x4* Wt = (u32x4*)(ws + WS_WT) + (q * NG4 + (k >> 2)) * 256 + a * 4 + (k & 3);
#pragma unroll
            for (int bq = 0; bq < 4; ++bq) Wt[bq * 64] = u32x4{o[4 * bq], o[4 * bq + 1], o[4 * bq + 2], o[4 * bq + 3]};
        }
        STAMP(0, 5); STAMP_FLUSH(0);
    } else if (b < 256 + K1_WT_BLOCKS + K1_W2_BLOCKS) {
        const int u = (b - 256 - K1_WT_BLOCKS) * 8 + wv;
        const int nt2 = u / 48, kb = u - nt2 * 48;
        char* lb = smem + wv * 5120;
        const unsigned l0 = (unsigned)(uintptr_t)(LAS char*)lb, l1 = l0 + 4096;
        unsigned wo[5];
#pragma unroll
        for (int r = 0; r < 5; ++r) {
            const int L = r * 64 + lane, row = L / 10, c = L - row * 10;
            wo[r] = (unsigned)(row * (D_ * 2) + c * 16 - (r & 3) * 1024);
        }
        const char* g = w2 + (size_t)kb * (32 * D_ * 2) + nt2 * 160;
        asm volatile("s_mov_b32 m0, %6\n\ts_nop 0\n\t"
                     "global_load_lds_dwordx4 %0, %5\n\t"
                     "global_load_lds_dwordx4 %1, %5 offset:1024\n\t"
                     "global_load_lds_dwordx4 %2, %5 offset:2048\n\t"
                     "global_load_lds_dwordx4 %3, %5 offset:3072\n\t"
                     "s_mov_b32 m0, %7\n\ts_nop 0\n\t"
                     "global_load_lds_dwordx4 %4, %5\n\t"
                     "s_waitcnt vmcnt(0)"
                     :: "v"(wo[0]), "v"(wo[1]), "v"(wo[2]), "v"(wo[3]), "v"(wo[4]), "s"(g), "s"(l0), "s"(l1) : "memory");
        const int gq = lane >> 4;
        const unsigned ra = (16 * (gq >> 1) + 4 * (gq & 1) + ((lane & 15) >> 2)) * 160 + (lane & 3) * 8;
        char* dst = ws + WS_W2T + (size_t)(nt2 * 48 + kb) * 5120 + lane * 16;
#pragma unroll
        for (int j = 0; j < 5; ++j) {
            s16x4 lo = __builtin_amdgcn_ds_read_tr16_b64_v4i16((LAS s16x4*)((LAS char*)lb + ra + j * 32));
            s16x4 hi = __builtin_amdgcn_ds_read_tr16_b64_v4i16((LAS s16x4*)((LAS char*)lb + ra + j * 32 + 8 * 160));
            *(s16x8*)(dst + j * 1024) = __builtin_shufflevector(lo, hi, 0, 1, 2, 3, 4, 5, 6, 7);
        }
        STAMP(0, 5); STAMP_FLUSH(0);
    } else {
        const int id = b - 256 - K1_WT_BLOCKS - K1_W2_BLOCKS;
        const int half = (id >> 2) & 1, part = (id >> 3) * 8 + wv;
        unsigned acc = 0;
#pragma unroll
        for (int i = 0; i < 12; ++i) {
            const int L = i * 64 + lane, row = L / 96, c = L - row * 96;
            const size_t off = (size_t)(8 * part + row) * (F_ * 2) + half * 1536 + c * 16;
            const u32x4 p = *(const u32x4*)(w1 + off), q = *(const u32x4*)(w3 + off);
            acc ^= p.x ^ q.x;
        }
        asm volatile("" :: "v"(acc));
    }
}

constexpr int WGRP = 3104, SLOT = 4 * WGRP, NS = 10, KSTEPS = 20;
constexpr int FSCR = 16 * 68 * 8;
constexpr int L2_F = NS * SLOT, L2_FLAG = L2_F + 3 * FSCR;
constexpr int K2_LDS = L2_FLAG + 64;

template <int N> DEV void wait_vm() { asm volatile("s_waitcnt vmcnt(%0)" ::"i"(N) : "memory"); }
DEV unsigned lds_min4(const char* smem, int off) {
    u32x4 c = *(const volatile LAS u32x4*)((LAS const char*)smem + off);
    unsigned a = c.x < c.y ? c.x : c.y, b = c.z < c.w ? c.z : c.w;
    return __builtin_amdgcn_readfirstlane(a < b ? a : b);
}
DEV unsigned lds_word(const char* smem, int off) { return __builtin_amdgcn_readfirstlane(*(const volatile LAS unsigned*)((LAS const char*)smem + off)); }
struct Dma { unsigned wo[3]; const char* w1b; const char* w3b; unsigned lw; char* smem; char* ws; };
constexpr int issued_before(int p) { int n = p == 0 ? 2 : p + 3; return n > KSTEPS ? KSTEPS : n; }
template <int ST> DEV void dma_stage(const Dma& d) {
    if constexpr (ST >= NS) {
        while (lds_min4(d.smem, L2_FLAG + 16) < (unsigned)(ST - NS + 1)) __builtin_amdgcn_s_sleep(1);
    }
#pragma unroll
    for (int ks = 0; ks < 2; ++ks) {
        const char* g1 = d.w1b + (size_t)(2 * ST + ks) * (16 * F_ * 2);
        const char* g3 = d.w3b + (size_t)(2 * ST + ks) * (16 * F_ * 2);
        const unsigned l0 = d.lw + (ST % NS) * SLOT + ks * (2 * WGRP), l1 = l0 + WGRP;
        asm volatile("s_mov_b32 m0, %5\n\ts_nop 0\n\t"
                     "global_load_lds_dwordx4 %0, %3\n\t"
                     "global_load_lds_dwordx4 %1, %3 offset:1024\n\t"
                     "global_load_lds_dwordx4 %2, %3 offset:2048\n\t"
                     "s_mov_b32 m0, %6\n\ts_nop 0\n\t"
                     "global_load_lds_dwordx4 %0, %4\n\t"
                     "global_load_lds_dwordx4 %1, %4 offset:1024\n\t"
                     "global_load_lds_dwordx4 %2, %4 offset:2048"
                     :: "v"(d.wo[0]), "v"(d.wo[1]), "v"(d.wo[2]), "s"(g1), "s"(g3), "s"(l0), "s"(l1) : "memory");
    }
}
template <int A, int B> DEV void dma_range(const Dma& d) { if constexpr (A < B) { dma_stage<A>(d); dma_range<A + 1, B>(d); } }
template <int P> DEV void dma_run(const Dma& d) {
    if constexpr (P < KSTEPS) {
        if constexpr (P > 0) dma_range<issued_before(P - 1), issued_before(P)>(d);
        wait_vm<12 * (issued_before(P) - 1 - P)>();
        *(volatile LAS unsigned*)((LAS char*)d.smem + L2_FLAG) = (unsigned)(P + 1);
        dma_run<P + 1>(d);
    }
}

DEV float silu_f(float v) { return v * __builtin_amdgcn_rcpf(1.0f + __builtin_amdgcn_exp2f(v * -1.44269504088896341f)); }
DEV u32x4 wfrag(const char* smem, unsigned a) {
    s16x4 lo = __builtin_amdgcn_ds_read_tr16_b64_v4i16((LAS s16x4*)((LAS char*)smem + a));
    s16x4 hi = __builtin_amdgcn_ds_read_tr16_b64_v4i16((LAS s16x4*)((LAS char*)smem + a + 768));
    return __builtin_bit_cast(u32x4, __builtin_shufflevector(lo, hi, 0, 1, 2, 3, 4, 5, 6, 7));
}

__global__ NOPK __launch_bounds__(512) void k2_kernel(const char* __restrict__ w1, const char* __restrict__ w3, char* __restrict__ ws) {
    extern __shared__ __attribute__((aligned(16))) char smem[];
    const int b = blockIdx.x, tid = threadIdx.x, lane = tid & 63;
    const int wv = __builtin_amdgcn_readfirstlane(tid >> 6);
    const int xcd = b & 7, idx = b >> 3;
    const int mt = (xcd & 3) * 4 + (idx & 3), nt = (xcd >> 2) * 8 + (idx >> 2);
    STAMP_DECL;
    STAMP(1, 0);
    if (wv == 0) {
        __builtin_amdgcn_s_setprio(2);
        Dma d;
#pragma unroll
        for (int r = 0; r < 3; ++r) {
            const int L = r * 64 + lane, row = L / 12, c = L - row * 12;
            d.wo[r] = (unsigned)(row * (F_ * 2) + c * 16 - r * 1024);
        }
        d.smem = smem; d.ws = ws;
        d.w1b = w1 + nt * 192; d.w3b = w3 + nt * 192;
        d.lw = (unsigned)(uintptr_t)(LAS char*)smem;
        if (tid < 16) *(volatile LAS unsigned*)((LAS char*)smem + L2_FLAG + tid * 4) = 0u;
        wait_lgkm0();
        dma_range<0, 1>(d);
        bar();
        dma_range<1, issued_before(0)>(d);
        STAMP(1, 1);
        dma_run<0>(d);
        STAMP(1, 11);
        char* sp = ws + WS_S + (size_t)b * 10240 + lane * 16;
#pragma unroll
        for (int i = 0; i < 10; ++i) st16_wt(sp + i * 1024, u32x4{CANARY, CANARY, CANARY, CANARY});
        STAMP(1, 12); STAMP_FLUSH(1);
    } else if (wv >= 4) {
        __builtin_amdgcn_s_setprio(3);
        const int mw = wv - 4;
        const int h = lane >> 5, r32 = lane & 31;
        const int l16 = lane * 16;
        const __amdgpu_buffer_rsrc_t xr = __builtin_amdgcn_make_buffer_rsrc((void*)(ws + WS_XN + (size_t)mt * (20 * 8192) + mw * 1024), 0, 20 * 8192, 0x00020000);
        u32x4 xf[5][2];
#pragma unroll
        for (int st = 0; st < 5; ++st) {
            xf[st][0] = __builtin_amdgcn_raw_buffer_load_b128(xr, l16, st * 8192, 0);
            xf[st][1] = __builtin_amdgcn_raw_buffer_load_b128(xr, l16, st * 8192 + 4096, 0);
            if (st == 0) bar();
        }
        f32x16 acc[6];
#pragma unroll
        for (int i = 0; i < 6; ++i)
#pragma unroll
            for (int r = 0; r < 16; ++r) acc[i][r] = 0.f;
        u32x4 wa[6], wb[6];
        unsigned rc = ((lane >> 4) & 1) * WGRP + (8 * h + ((lane & 15) >> 2)) * 192 + (lane & 3) * 8, rn = rc + 5 * SLOT;
        unsigned fa = L2_FLAG, da = L2_FLAG + 16 + mw * 4, dv = 0;
        PIN(rc); PIN(rn); PIN(fa); PIN(da); PIN(dv);
#define MM(i, w, x) asm volatile("v_mfma_f32_32x32x16_bf16 %0, %1, %2, %0" : "+v"(acc[i]) : "v"(w), "v"(x) : "memory")
        while (lds_word(smem, L2_FLAG) < 1u) __builtin_amdgcn_s_sleep(1);
        STAMP(1, 1);
#pragma unroll
        for (int i = 0; i < 6; ++i) wa[i] = wfrag(smem, rc + i * 32);
#pragma unroll 1
        for (int io = 0; io < 3; ++io) {
#pragma unroll
            for (int ii = 0; ii < 5; ++ii) {
                const int it = io * 5 + ii;
                const unsigned cb = rc + ii * SLOT + 2 * WGRP, nb = ii < 4 ? rc + (ii + 1) * SLOT : rn;
                MM(0, wa[0], xf[ii][0]);
                wb[0] = wfrag(smem, cb); wb[1] = wfrag(smem, cb + 32);
                MM(1, wa[1], xf[ii][0]);
                wb[2] = wfrag(smem, cb + 64); wb[3] = wfrag(smem, cb + 96);
                MM(2, wa[2], xf[ii][0]);
                wb[4] = wfrag(smem, cb + 128); wb[5] = wfrag(smem, cb + 160);
                unsigned fl = *(const volatile LAS unsigned*)((LAS const char*)smem + fa);
                MM(3, wa[3], xf[ii][0]);
                asm volatile("v_add_u32 %0, 1, %0" : "+v"(dv));
                MM(4, wa[4], xf[ii][0]);
                MM(5, wa[5], xf[ii][0]);
                {
                    unsigned m = __builtin_amdgcn_readfirstlane(fl);
                    const unsigned need = it == 14 ? KSTEPS : it + 2;
                    while (m < need) { __builtin_amdgcn_s_sleep(1); m = lds_word(smem, L2_FLAG); }
                }
                MM(0, wb[0], xf[ii][1]);
                wa[0] = wfrag(smem, nb); wa[1] = wfrag(smem, nb + 32);
                MM(1, wb[1], xf[ii][1]);
                wa[2] = wfrag(smem, nb + 64); wa[3] = wfrag(smem, nb + 96);
                MM(2, wb[2], xf[ii][1]);
                wa[4] = wfrag(smem, nb + 128); wa[5] = wfrag(smem, nb + 160);
                if ((it & 1) && io < 2) *(volatile LAS unsigned*)((LAS char*)smem + da) = dv;
                MM(3, wb[3], xf[ii][1]);
                xf[ii][0] = __builtin_amdgcn_raw_buffer_load_b128(xr, l16, (it + 5) * 8192, 0);
                MM(4, wb[4], xf[ii][1]);
                MM(5, wb[5], xf[ii][1]);
                xf[ii][1] = __builtin_amdgcn_raw_buffer_load_b128(xr, l16, (it + 5) * 8192 + 4096, 0);
            }
            { const unsigned t = rc; rc = rn; rn = t; }
            STAMP(1, 2 + io); CSTAMP(1, 2 + io);
        }
        char* ht = ws + WS_HT + (size_t)((((mt * 2 + (mw >> 1)) * 48 + 3 * nt) * 4 + 2 * (mw & 1) + (r32 >> 4)) * 64 + (r32 & 15) + 16 * h) * 16;
        u32x4 g0[9], g1[9];
#define TFRAG(i, n) wfrag(smem, rc + ((n) >> 1) * SLOT + ((n) & 1) * (2 * WGRP) + (i) * 32)
#pragma unroll
        for (int n = 1; n < 10; ++n) g0[n - 1] = TFRAG(0, n);
#pragma unroll
        for (int i = 0; i < 7; ++i) {
            float hh[8];
#pragma unroll
            for (int n = 0; n < 10; ++n) {
                if (i < 6) {
                    if (n == 0) MM(i, wa[i], xf[0][0]);
                    else if (i & 1) MM(i, g1[n - 1], xf[n >> 1][n & 1]);
                    else MM(i, g0[n - 1], xf[n >> 1][n & 1]);
                    if (i < 5 && n < 9) { if (i & 1) g0[n] = TFRAG(i + 1, n + 1); else g1[n] = TFRAG(i + 1, n + 1); }
                } else if (n == 0) {
                    asm volatile("s_nop 15\n\ts_nop 7" ::: "memory");
                }
                if (i > 0) {
                    if (n >= 1 && n <= 8) { hh[n - 1] = silu_f(acc[i - 1][n - 1]) * acc[i - 1][8 + n - 1]; PIN(hh[n - 1]); }
                    if (n == 9) {
                        const u32x4 o = {pack_bf16(hh[0], hh[1]), pack_bf16(hh[2], hh[3]), pack_bf16(hh[4], hh[5]), pack_bf16(hh[6], hh[7])};
                        st16_wt(ht + ((i - 1) >> 1) * 4096 + ((i - 1) & 1) * 512, o);
                    }
                }
                SB();
            }
        }
        STAMP(1, 12); STAMP_FLUSH(1);
    } else {
        const int fw = wv - 1;
        const int q = xcd, g4 = fw * 32 + idx;
        if (g4 >= NG4) return;
        bar();
        const int t = lane >> 2;
        cf tw[16];
#pragma unroll
        for (int a = 1; a < 16; ++a) { tw[a] = twid((float)(t * a) * (1.0f / 256.0f)); PIN(tw[a].x); PIN(tw[a].y); }
        SB();
        while (lds_word(smem, L2_FLAG) < 4u) __builtin_amdgcn_s_sleep(2);
        const int eo = (t * 8 + q) * TP + 4 * g4 + (lane & 3);
        const __amdgpu_buffer_rsrc_t tr = __builtin_amdgcn_make_buffer_rsrc((void*)(ws + WS_T16), 0, 2048 * TP * 4, 0x00020000);
        const __amdgpu_buffer_rsrc_t vr = __builtin_amdgcn_make_buffer_rsrc((void*)(ws + WS_T), 0, 2048 * TP * 8, 0x00020000);
        unsigned tv[16];
        u32x4 wq[4];
#pragma unroll
        for (int i = 0; i < 16; ++i) tv[i] = __builtin_amdgcn_raw_buffer_load_b32(tr, eo * 4, i * (128 * TP * 4), 0);
#pragma unroll
        for (int i = 0; i < 4; ++i) wq[i] = ((const u32x4*)(ws + WS_WT))[(size_t)(q * NG4 + g4) * 256 + i * 64 + lane];
        SB();
        char* fs = smem + L2_F + fw * FSCR;
        cf v[16];
#pragma unroll
        for (int i = 0; i < 16; ++i) { const f16x2 hv = __builtin_bit_cast(f16x2, tv[i]); v[i] = {(float)hv.x, (float)hv.y}; }
        STAMP(1, 1);
        fft16_a<-1>(v); fft16_t<-1>(v); fft16_b<-1>(v);
        lds_st(fs, lane, v[0]);
#pragma unroll
        for (int a = 1; a < 16; ++a) lds_st(fs, a * 68 + lane, cmul(v[a], tw[a]));
        wave_fence();
#pragma unroll
        for (int i = 0; i < 16; ++i) v[i] = lds_ld(fs, t * 68 + i * 4 + (lane & 3));
        fft16_a<-1>(v); fft16_t<-1>(v); fft16_b<-1>(v);
#pragma unroll
        for (int i = 0; i < 16; ++i) { const unsigned w = wq[i >> 2][i & 3]; v[i] = cmul(v[i], cf{bf_lo(w), bf_hi(w)}); }
        fft16_a<1>(v); fft16_t<1>(v); fft16_b<1>(v);
        wave_fence();
        lds_st(fs, t * 68 + (lane & 3), v[0]);
#pragma unroll
        for (int i = 1; i < 16; ++i) lds_st(fs, t * 68 + i * 4 + (lane & 3), cmulc(v[i], tw[i]));
        wave_fence();
#pragma unroll
        for (int a = 0; a < 16; ++a) v[a] = lds_ld(fs, a * 68 + lane);
        fft16_a<1>(v); fft16_t<1>(v); fft16_b<1>(v);
#pragma unroll
        for (int i = 0; i < 16; ++i)
            __builtin_amdgcn_raw_buffer_store_b64(__builtin_bit_cast(u32x2, f32x2{v[i].x, v[i].y}), vr, eo * 8, i * (128 * TP * 8), 0);
        STAMP(1, 12); STAMP_FLUSH(1);
    }
}

constexpr int RPITCH = 336;
constexpr int L3_RED = 0;
constexpr int L3_A = 4 * 64 * RPITCH;
constexpr int L3_B = L3_A + 4 * 720 * 8;
constexpr int L3_CNT = L3_B + 4 * 656 * 8;
constexpr int K3_LDS = L3_CNT + 64;
#define GO_STEP 9

DEV void helper_barrier(char* smem, int lane, unsigned target) {
    wait_lgkm0();
    LAS unsigned* cnt = (LAS unsigned*)((LAS char*)smem + L3_CNT);
    if (lane == 0) __hip_atomic_fetch_add(cnt, 1u, __ATOMIC_RELAXED, __HIP_MEMORY_SCOPE_WORKGROUP);
    while (__hip_atomic_load(cnt, __ATOMIC_RELAXED, __HIP_MEMORY_SCOPE_WORKGROUP) < target) {}
    asm volatile("" ::: "memory");
}
#define MFMA16(a, b, c) __builtin_amdgcn_mfma_f32_16x16x32_bf16(__builtin_bit_cast(bf16x8, a), __builtin_bit_cast(bf16x8, b), c, 0, 0, 0)

__global__ NOPK __launch_bounds__(768) void k3_kernel(const char* __restrict__ x, char* __restrict__ ws, char* __restrict__ out) {
    extern __shared__ __attribute__((aligned(16))) char smem[];
    const int b = blockIdx.x, tid = threadIdx.x, lane = tid & 63;
    const int wv = __builtin_amdgcn_readfirstlane(tid >> 6);
    const int xcd = b & 7, idx = b >> 3;
    const int mt2 = (xcd & 3) * 8 + (idx & 7), nt2 = (xcd >> 2) * 4 + (idx >> 3);
    STAMP_DECL;
    STAMP(2, 0);
    if (wv >= 8) {
        const int gw = wv - 8;
        const char* hb = ws + WS_HT + (size_t)(mt2 * 48 + 12 * gw) * 4096;
        const char* wb = ws + WS_W2T + (size_t)(nt2 * 48 + 12 * gw) * 5120;
        const int l16 = lane * 16;
        const __amdgpu_buffer_rsrc_t hr = __builtin_amdgcn_make_buffer_rsrc((void*)hb, 0, 12 * 4096, 0x00020000);
        const __amdgpu_buffer_rsrc_t wr_ = __builtin_amdgcn_make_buffer_rsrc((void*)wb, 0, 12 * 5120, 0x00020000);
#define HLD(st, tb) __builtin_amdgcn_raw_buffer_load_b128(hr, l16 + (tb) * 1024, (st) * 4096, 0)
#define WLD(st, j) __builtin_amdgcn_raw_buffer_load_b128(wr_, l16 + ((j) & 3) * 1024, (st) * 5120 + ((j) >> 2) * 4096, 0)
        u32x4 hf[2][4], wf[2][5];
        bar();
#pragma unroll
        for (int st = 0; st < 2; ++st) {
#pragma unroll
            for (int tb = 0; tb < 4; ++tb) hf[st][tb] = HLD(st, tb);
#pragma unroll
            for (int j = 0; j < 5; ++j) wf[st][j] = WLD(st, j);
            SB();
        }
        f32x4 acc[5][4];
#pragma unroll
        for (int j = 0; j < 5; ++j)
#pragma unroll
            for (int tb = 0; tb < 4; ++tb) acc[j][tb] = f32x4{0.f, 0.f, 0.f, 0.f};
        char* rp = smem + L3_RED + gw * (64 * RPITCH) + (lane & 15) * RPITCH + (lane >> 4) * 16;
        SB();
#pragma unroll
        for (int st = 0; st < 12; ++st) {
            const int sl = st & 1;
            const bool more = st + 2 < 12;
#pragma unroll
            for (int j = 0; j < 4; ++j) {
#pragma unroll
                for (int tb = 0; tb < 4; ++tb) acc[j][tb] = MFMA16(wf[sl][j], hf[sl][tb], acc[j][tb]);
                SB();
                if (more) wf[sl][j] = WLD(st + 2, j);
                if (st == 11 && j > 0) {
#pragma unroll
                    for (int tb = 0; tb < 4; ++tb) *(LAS f32x4*)((LAS char*)rp + tb * (16 * RPITCH) + (j - 1) * 64) = acc[j - 1][tb];
                }
                SB();
            }
#pragma unroll
            for (int tb = 0; tb < 4; ++tb) {
                acc[4][tb] = MFMA16(wf[sl][4], hf[sl][tb], acc[4][tb]);
                SB();
                if (more) hf[sl][tb] = HLD(st + 2, tb);
                SB();
            }
            if (more) wf[sl][4] = WLD(st + 2, 4);
            if (st == GO_STEP) *(volatile LAS unsigned*)((LAS char*)smem + L3_CNT + 4) = 1u;
            SB();
        }
        STAMP(2, 1);
#pragma unroll
        for (int j = 3; j < 5; ++j)
#pragma unroll
            for (int tb = 0; tb < 4; ++tb) *(LAS f32x4*)((LAS char*)rp + tb * (16 * RPITCH) + j * 64) = acc[j][tb];
        wait_lgkm0();
        STAMP(2, 2);
        bar();
        STAMP_FLUSH(2);
    } else {
        __builtin_amdgcn_s_setprio(2);
        const int s0 = (b & 7) * 32 + (b >> 3);
        const f32x2* V = (const f32x2*)(ws + WS_T) + (size_t)s0 * (8 * TP);
        char* sg = ws + WS_S;
        f32x2 u[10], u2[10], e0, e1;
        if (wv < 4) {
            const int qa = wv, qb = wv == 0 ? 0 : 8 - wv;
#pragma unroll
            for (int bb = 0; bb < 5; ++bb) u[bb] = V[qa * TP + lane + 64 * bb];
#pragma unroll
            for (int bb = 5; bb < 10; ++bb) u[bb] = V[qb * TP + 640 - lane - 64 * bb];
            if (wv == 0) {
#pragma unroll
                for (int bb = 0; bb < 5; ++bb) u2[bb] = V[4 * TP + lane + 64 * bb];
#pragma unroll
                for (int bb = 5; bb < 10; ++bb) u2[bb] = V[4 * TP + 640 - lane - 64 * bb];
            } else {
                e0 = V[qb * TP];
                e1 = V[qa * TP + 320];
            }
        }
        const int pc0 = tid, pc1 = wv >= 6 ? tid + 128 : tid;
        const int r0 = pc0 / 10, c0 = pc0 - r0 * 10, r1 = pc1 / 10, c1 = pc1 - r1 * 10;
        const size_t eo0 = (size_t)(mt2 * 64 + r0) * (D_ * 2) + nt2 * 160 + c0 * 16, eo1 = (size_t)(mt2 * 64 + r1) * (D_ * 2) + nt2 * 160 + c1 * 16;
        const u32x4 xa0 = *(const u32x4*)(x + eo0);
        const u32x4 xa1 = *(const u32x4*)(x + eo1);
        if (tid == 320) *(LAS u32x2*)((LAS char*)smem + L3_CNT) = u32x2{0u, 0u};
        wait_lgkm0();
        bar();
        STAMP(2, 12);
        if (wv < 5) {
            constexpr float NORM = 1.0f / (2048.0f * 640.0f);
            const int qr = tid / 80, r = tid - qr * 80;
            cf t80[10], t640[8];
#pragma unroll
            for (int t = 1; t < 10; ++t) { t80[t] = twid((float)((lane >> 3) * t) * (1.0f / 80.0f)); PIN(t80[t].x); PIN(t80[t].y); }
#pragma unroll
            for (int a = 1; a < 8; ++a) { t640[a] = twid((float)(r * a) * (1.0f / 640.0f)); PIN(t640[a].x); PIN(t640[a].y); }
            cf twq = twid((float)(s0 * (lane & 7)) * (1.0f / 2048.0f));
            SB();
            STAMP(2, 11);
            char* bufA = smem + L3_A;
            char* bufB = smem + L3_B;
            if (wv < 4) {
                cf f[10];
                if (wv == 0) {
                    const cf w4 = {rdlane(twq.x, 4) * NORM, rdlane(twq.y, 4) * NORM};
#pragma unroll
                    for (int bb = 0; bb < 10; ++bb) {
                        cf A = {u[bb].x * NORM, u[bb].y * NORM}, B = cmulc(cf{u2[bb].x, u2[bb].y}, w4);
                        if (bb == 0 || bb == 5) { if (lane == 0) { A.y = 0.f; B.y = 0.f; } }
                        f[bb] = bb < 5 ? cf{A.x - B.y, A.y + B.x} : cf{A.x + B.y, B.x - A.y};
                    }
                } else {
                    const float N2 = 2.0f * NORM;
                    const cf wa = {rdlane(twq.x, wv) * N2, rdlane(twq.y, wv) * N2}, wb = {rdlane(twq.x, 8 - wv) * N2, rdlane(twq.y, 8 - wv) * N2};
#pragma unroll
                    for (int bb = 0; bb < 5; ++bb) f[bb] = cmulc(cf{u[bb].x, u[bb].y}, wa);
#pragma unroll
                    for (int bb = 5; bb < 10; ++bb) { cf t = cmulc(cf{u[bb].x, u[bb].y}, wb); f[bb] = cf{t.x, -t.y}; }
                    const cf g0 = cmulc(cf{e0.x, e0.y}, wb), g1 = cmulc(cf{e1.x, e1.y}, wa);
                    if (lane == 0) {
                        f[0] = cf{0.5f * (f[0].x + g0.x), 0.5f * (f[0].y - g0.y)};
                        f[5] = cf{0.5f * (f[5].x + g1.x), 0.5f * (f[5].y + g1.y)};
                    }
                }
                STAMP(2, 1);
                fft10<1>(f);
                lds_st(bufA, wv * 720 + lane, f[0]);
#pragma unroll
                for (int t = 1; t < 10; ++t) lds_st(bufA, wv * 720 + t * 72 + lane, cmulc(f[t], t80[t]));
            }
            STAMP(2, 2);
            helper_barrier(smem, lane, 5);
            STAMP(2, 13);
            cf a8[8];
            {
                const int base = qr * 720 + (r >> 3) * 72 + (r & 7);
#pragma unroll
                for (int i = 0; i < 8; ++i) a8[i] = lds_ld(bufA, base + 8 * i);
                fft8<1>(a8);
                const int ob = qr * 656 + (r & 7) * 82 + (r >> 3);
#pragma unroll
                for (int i = 0; i < 8; ++i) lds_st(bufB, ob + 10 * i, a8[i]);
            }
            STAMP(2, 14);
            helper_barrier(smem, lane, 10);
            STAMP(2, 15);
            {
                a8[0] = lds_ld(bufB, qr * 656 + r);
#pragma unroll
                for (int a = 1; a < 8; ++a) a8[a] = cmulc(lds_ld(bufB, qr * 656 + a * 82 + r), t640[a]);
                fft8<1>(a8);
#pragma unroll
                for (int i = 0; i < 8; ++i) lds_st(bufA, qr * 720 + r + 80 * i, a8[i]);
            }
            helper_barrier(smem, lane, 15);
            STAMP(2, 3);
            {
                f32x4 p[4];
#pragma unroll
                for (int q = 0; q < 4; ++q) p[q] = *(const LAS f32x4*)((LAS const char*)bufA + (q * 720 + 2 * tid) * 8);
                float y[2][8];
#pragma unroll
                for (int e = 0; e < 2; ++e) {
                    const float P0 = p[0][2 * e], P4 = p[0][2 * e + 1];
                    const float A = P0 + P4, B = P0 - P4;
                    const float c0 = A + p[2][2 * e], c2 = A - p[2][2 * e], c1 = B - p[2][2 * e + 1], c3 = B + p[2][2 * e + 1];
                    const float o0 = p[1][2 * e] + p[3][2 * e], o2 = p[3][2 * e + 1] - p[1][2 * e + 1];
                    const float uu = p[1][2 * e] - p[3][2 * e], vv = p[1][2 * e + 1] + p[3][2 * e + 1];
                    const float o1 = uu - vv, o3 = -uu - vv;
                    y[e][0] = c0 + o0; y[e][4] = c0 - o0;
                    y[e][2] = c2 + o2; y[e][6] = c2 - o2;
                    y[e][1] = c1 + RH * o1; y[e][5] = c1 - RH * o1;
                    y[e][3] = c3 + RH * o3; y[e][7] = c3 - RH * o3;
                }
                char* stg = bufB + wv * 2048;
#pragma unroll
                for (int j = 0; j < 8; ++j) *(LAS unsigned*)((LAS char*)stg + j * 256 + lane * 4) = pack_bf16(y[0][j], y[1][j]);
                wave_fence();
#pragma unroll
                for (int hh = 0; hh < 2; ++hh) {
                    const int j = 4 * hh + (lane >> 4);
                    u32x4 o = *(const LAS u32x4*)((LAS const char*)stg + hh * 1024 + lane * 16);
                    st16_wt(sg + (size_t)(s0 + 256 * j) * (D_ * 2) + wv * 256 + (lane & 15) * 16, o);
                }
            }
            STAMP(2, 4);
        }
        while (lds_word(smem, L3_CNT + 4) == 0u) __builtin_amdgcn_s_sleep(4);
        STAMP(2, 7);
        u32x4 sa0, sa1;
        {
            const char* src0 = sg + eo0;
            const char* src1 = sg + eo1;
            for (unsigned spin = 0; spin < (1u << 20); ++spin) {
                asm volatile("global_load_dwordx4 %0, %2, off sc1\n\tglobal_load_dwordx4 %1, %3, off sc1\n\ts_waitcnt vmcnt(0)"
                             : "=&v"(sa0), "=&v"(sa1) : "v"(src0), "v"(src1) : "memory");
                const bool bad = sa0.x == CANARY || sa0.y == CANARY || sa0.z == CANARY || sa0.w == CANARY ||
                                 sa1.x == CANARY || sa1.y == CANARY || sa1.z == CANARY || sa1.w == CANARY;
                if (__builtin_amdgcn_ballot_w64(bad) == 0ull) break;
                __builtin_amdgcn_s_sleep(8);
            }
        }
        STAMP(2, 8);
        bar();
        STAMP(2, 9);
        for (int rep = 0; rep < (wv >= 6 ? 2 : 1); ++rep) {
            const char* rp = smem + L3_RED + (rep ? r1 : r0) * RPITCH + (rep ? c1 : c0) * 32;
            f32x4 o0 = *(const LAS f32x4*)((LAS const char*)rp), o1 = *(const LAS f32x4*)((LAS const char*)rp + 16);
#pragma unroll
            for (int w = 1; w < 4; ++w) {
                o0 += *(const LAS f32x4*)((LAS const char*)rp + w * (64 * RPITCH));
                o1 += *(const LAS f32x4*)((LAS const char*)rp + w * (64 * RPITCH) + 16);
            }
            const u32x4 sv = rep ? sa1 : sa0, xv = rep ? xa1 : xa0;
            const float o[8] = {o0.x, o0.y, o0.z, o0.w, o1.x, o1.y, o1.z, o1.w};
            u32x4 res;
#pragma unroll
            for (int i = 0; i < 4; ++i)
                res[i] = pack_bf16(bf_lo(xv[i]) + bf_lo(sv[i]) * silu_f(o[2 * i]), bf_hi(xv[i]) + bf_hi(sv[i]) * silu_f(o[2 * i + 1]));
            *(u32x4*)(out + (rep ? eo1 : eo0)) = res;
        }
        STAMP(2, 10); STAMP_FLUSH(2);
    }
}

extern "C" void kernel_launch(void* const* d_in, const int* in_sizes, int n_in, void* d_out, int out_size, void* d_ws,
                              size_t ws_size, hipStream_t stream) {
    (void)in_sizes; (void)n_in; (void)out_size; (void)ws_size;
    char* ws = (char*)d_ws;
    (void)hipFuncSetAttribute((const void*)k2_kernel, hipFuncAttributeMaxDynamicSharedMemorySize, K2_LDS);
    (void)hipFuncSetAttribute((const void*)k3_kernel, hipFuncAttributeMaxDynamicSharedMemorySize, K3_LDS);
    k1_kernel<<<256 + K1_WT_BLOCKS + K1_W2_BLOCKS + K1_WARM_BLOCKS, 512, 0, stream>>>(
        (const unsigned*)d_in[0], (const unsigned short*)d_in[1], (const unsigned short*)d_in[2], (const char*)d_in[4],
        (const char*)d_in[5], (const char*)d_in[6], ws);
    k2_kernel<<<256, 512, K2_LDS, stream>>>((const char*)d_in[4], (const char*)d_in[5], ws);
    k3_kernel<<<256, 768, K3_LDS, stream>>>((const char*)d_in[0], ws, (char*)d_out);
}
```

```cpp
#include <hip/hip_runtime.h>
#include <hip/hip_bf16.h>
#include <stdint.h>

typedef float f32x2 __attribute__((ext_vector_type(2)));
typedef float f32x4 __attribute__((ext_vector_type(4)));
typedef float f32x16 __attribute__((ext_vector_type(16)));
typedef short s16x4 __attribute__((ext_vector_type(4)));
typedef short s16x8 __attribute__((ext_vector_type(8)));
typedef __bf16 bf16x2 __attribute__((ext_vector_type(2)));
typedef __bf16 bf16x8 __attribute__((ext_vector_type(8)));
typedef _Float16 f16x2 __attribute__((ext_vector_type(2)));
typedef unsigned u32x2 __attribute__((ext_vector_type(2)));
typedef unsigned u32x4 __attribute__((ext_vector_type(4)));

#define DEV __device__ __forceinline__
#define LAS __attribute__((address_space(3)))
#if defined(__HIP_DEVICE_COMPILE__)
#define NOPK __attribute__((target("no-packed-fp32-ops")))
#else
#define NOPK
#endif

constexpr int S_ = 2048, D_ = 640, F_ = 1536, DF_ = 321;
constexpr int NG4 = 81;
constexpr int NUNIT = 8 * NG4;

constexpr size_t WS_XN = 0;
constexpr size_t WS_T = 4u << 20;
constexpr size_t WS_WT = 12u << 20;
constexpr size_t WS_W2T = 16u << 20;
constexpr size_t WS_HT = 20u << 20;
constexpr size_t WS_S = 28u << 20;
constexpr size_t WS_T16 = 32u << 20;
constexpr size_t WS_HINT = 36u << 20;
constexpr int TP = 328;

constexpr size_t WS_DBG = 64u << 20;
#define STAMP_DECL
#define STAMP(kern, id) do {} while (0)
#define CSTAMP(kern, id) do {} while (0)
#define STAMP_FLUSH(kern) do {} while (0)

struct cf { float x, y; };
DEV cf operator+(cf a, cf b) { return {a.x + b.x, a.y + b.y}; }
DEV cf operator-(cf a, cf b) { return {a.x - b.x, a.y - b.y}; }
DEV cf cmul(cf a, cf w) { return {a.x * w.x - a.y * w.y, a.x * w.y + a.y * w.x}; }
DEV cf cmulc(cf a, cf w) { return {a.x * w.x + a.y * w.y, a.y * w.x - a.x * w.y}; }
template <int DIR> DEV cf muli(cf a) { return DIR > 0 ? cf{-a.y, a.x} : cf{a.y, -a.x}; }
DEV cf twid(float f) { return {__builtin_amdgcn_cosf(f), -__builtin_amdgcn_sinf(f)}; }

DEV unsigned pack_bf16(float a, float b) {
    f32x2 v = {a, b};
    return __builtin_bit_cast(unsigned, __builtin_convertvector(v, bf16x2));
}
DEV float bf_lo(unsigned u) { return __uint_as_float(u << 16); }
DEV float bf_hi(unsigned u) { return __uint_as_float(u & 0xffff0000u); }
DEV float rbf(float a) { return bf_lo(pack_bf16(a, a)); }

template <int DIR> DEV void fft4(cf& a0, cf& a1, cf& a2, cf& a3) {
    cf t0 = a0 + a2, t1 = a0 - a2, t2 = a1 + a3, t3 = muli<DIR>(a1 - a3);
    a0 = t0 + t2; a2 = t0 - t2; a1 = t1 + t3; a3 = t1 - t3;
}
constexpr float RH = 0.70710678118654752f;
template <int DIR> DEV cf mulw8(cf a) { return DIR > 0 ? cf{(a.x - a.y) * RH, (a.x + a.y) * RH} : cf{(a.x + a.y) * RH, (a.y - a.x) * RH}; }
template <int DIR> DEV cf mulw83(cf a) { return DIR > 0 ? cf{-(a.x + a.y) * RH, (a.x - a.y) * RH} : cf{(a.y - a.x) * RH, -(a.x + a.y) * RH}; }

template <int DIR> DEV void fft8(cf (&a)[8]) {
    fft4<DIR>(a[0], a[2], a[4], a[6]);
    fft4<DIR>(a[1], a[3], a[5], a[7]);
    cf o1 = mulw8<DIR>(a[3]), o2 = muli<DIR>(a[5]), o3 = mulw83<DIR>(a[7]);
    cf e0 = a[0], e1 = a[2], e2 = a[4], e3 = a[6], o0 = a[1];
    a[0] = e0 + o0; a[4] = e0 - o0;
    a[1] = e1 + o1; a[5] = e1 - o1;
    a[2] = e2 + o2; a[6] = e2 - o2;
    a[3] = e3 + o3; a[7] = e3 - o3;
}
template <int DIR> DEV void fft5(cf (&a)[5]) {
    constexpr float c1 = 0.30901699437494742f, c2 = -0.80901699437494742f;
    constexpr float s1 = 0.95105651629515357f, s2 = 0.58778525229247313f;
    cf t1 = a[1] + a[4], t2 = a[2] + a[3], t3 = a[1] - a[4], t4 = a[2] - a[3];
    cf m1 = {a[0].x + c1 * t1.x + c2 * t2.x, a[0].y + c1 * t1.y + c2 * t2.y};
    cf m2 = {a[0].x + c2 * t1.x + c1 * t2.x, a[0].y + c2 * t1.y + c1 * t2.y};
    cf n1 = muli<DIR>(cf{s1 * t3.x + s2 * t4.x, s1 * t3.y + s2 * t4.y});
    cf n2 = muli<DIR>(cf{s2 * t3.x - s1 * t4.x, s2 * t3.y - s1 * t4.y});
    a[0] = a[0] + t1 + t2;
    a[1] = m1 + n1; a[4] = m1 - n1;
    a[2] = m2 + n2; a[3] = m2 - n2;
}
template <int DIR> DEV void fft10(cf (&a)[10]) {
    cf e[5], o[5];
#pragma unroll
    for (int n2 = 0; n2 < 5; ++n2) { const cf p = a[(2 * n2) % 10], q = a[(2 * n2 + 5) % 10]; e[n2] = p + q; o[n2] = p - q; }
    fft5<DIR>(e); fft5<DIR>(o);
#pragma unroll
    for (int k2 = 0; k2 < 5; ++k2) { a[(6 * k2) % 10] = e[k2]; a[(6 * k2 + 5) % 10] = o[k2]; }
}
template <int DIR> DEV void fft16_a(cf (&a)[16]) {
#pragma unroll
    for (int n2 = 0; n2 < 4; ++n2) fft4<DIR>(a[n2], a[n2 + 4], a[n2 + 8], a[n2 + 12]);
}
template <int DIR> DEV void fft16_t(cf (&a)[16]) {
    constexpr float C1 = 0.92387953251128674f, S1 = 0.38268343236508977f;
    const cf w1 = {C1, DIR * S1}, w3 = {S1, DIR * C1}, w9 = {-C1, -DIR * S1};
    a[1 + 4] = cmul(a[1 + 4], w1);
    a[1 + 8] = mulw8<DIR>(a[1 + 8]);
    a[1 + 12] = cmul(a[1 + 12], w3);
    a[2 + 4] = mulw8<DIR>(a[2 + 4]);
    a[2 + 8] = muli<DIR>(a[2 + 8]);
    a[2 + 12] = mulw83<DIR>(a[2 + 12]);
    a[3 + 4] = cmul(a[3 + 4], w3);
    a[3 + 8] = mulw83<DIR>(a[3 + 8]);
    a[3 + 12] = cmul(a[3 + 12], w9);
}
template <int DIR> DEV void fft16_b(cf (&a)[16]) {
#pragma unroll
    for (int k1 = 0; k1 < 4; ++k1) fft4<DIR>(a[4 * k1], a[4 * k1 + 1], a[4 * k1 + 2], a[4 * k1 + 3]);
    cf b[16];
#pragma unroll
    for (int k1 = 0; k1 < 4; ++k1)
#pragma unroll
        for (int k2 = 0; k2 < 4; ++k2) b[k1 + 4 * k2] = a[4 * k1 + k2];
#pragma unroll
    for (int i = 0; i < 16; ++i) a[i] = b[i];
}

template <int CTRL, int RM> DEV float dpp_add(float v) {
    int t = __builtin_amdgcn_update_dpp(0, __float_as_int(v), CTRL, RM, 0xf, true);
    return v + __int_as_float(t);
}
DEV float wave_sum(float v) {
    v = dpp_add<0x111, 0xf>(v);
    v = dpp_add<0x112, 0xf>(v);
    v = dpp_add<0x114, 0xf>(v);
    v = dpp_add<0x118, 0xf>(v);
    v = dpp_add<0x142, 0xa>(v);
    v = dpp_add<0x143, 0xc>(v);
    return __int_as_float(__builtin_amdgcn_readlane(__float_as_int(v), 63));
}
DEV float rdlane(float v, int l) { return __int_as_float(__builtin_amdgcn_readlane(__float_as_int(v), l)); }
#define PIN(x) asm volatile("" : "+v"(x))
#define SB() __builtin_amdgcn_sched_barrier(0)
DEV void bar() { SB(); __builtin_amdgcn_s_barrier(); asm volatile("" ::: "memory"); SB(); }
DEV void wait_lgkm0() { asm volatile("s_waitcnt lgkmcnt(0)" ::: "memory"); }
DEV void st16_wt(void* p, u32x4 v) { asm volatile("global_store_dwordx4 %0, %1, off sc1\n\ts_nop 1" ::"v"(p), "v"(v) : "memory"); }
DEV void st8_wt(void* p, f32x2 v) { asm volatile("global_store_dwordx2 %0, %1, off sc1" ::"v"(p), "v"(v) : "memory"); }
DEV int s0_of(int b) { return (b & 3) * 64 + ((b >> 2) & 1) * 32 + (b >> 3); }
DEV void wave_fence() { asm volatile("" ::: "memory"); __builtin_amdgcn_wave_barrier(); }

DEV cf lds_ld(const char* base, int idx) { f32x2 v = *(const LAS f32x2*)((LAS const char*)base + idx * 8); return {v.x, v.y}; }
DEV void lds_st(char* base, int idx, cf a) { f32x2 v = {a.x, a.y}; *(LAS f32x2*)((LAS char*)base + idx * 8) = v; }

typedef f32x2 c2;
DEV c2 addi(c2 a, c2 b) { c2 r; asm("v_pk_add_f32 %0, %1, %2 op_sel:[0,1] op_sel_hi:[1,0] neg_lo:[0,1]" : "=v"(r) : "v"(a), "v"(b)); return r; }
DEV c2 subi(c2 a, c2 b) { c2 r; asm("v_pk_add_f32 %0, %1, %2 op_sel:[0,1] op_sel_hi:[1,0] neg_hi:[0,1]" : "=v"(r) : "v"(a), "v"(b)); return r; }
DEV c2 addc(c2 a, c2 b) { c2 r; asm("v_pk_add_f32 %0, %1, %2 neg_hi:[0,1]" : "=v"(r) : "v"(a), "v"(b)); return r; }
DEV c2 subc(c2 a, c2 b) { c2 r; asm("v_pk_add_f32 %0, %1, %2 neg_lo:[0,1]" : "=v"(r) : "v"(a), "v"(b)); return r; }
DEV c2 pmul(c2 a, c2 w) {
    c2 t = a.xx * w, r;
    asm("v_pk_fma_f32 %0, %1, %2, %3 op_sel:[1,1,0] op_sel_hi:[1,0,1] neg_lo:[0,1,0]" : "=v"(r) : "v"(a), "v"(w), "v"(t));
    return r;
}
DEV c2 pmulc(c2 a, c2 w) {
    c2 t = a.xx * w, r;
    asm("v_pk_fma_f32 %0, %1, %2, %3 op_sel:[1,1,0] op_sel_hi:[1,0,1] neg_hi:[0,1,0]" : "=v"(r) : "v"(a), "v"(w), "v"(t));
    return r;
}
DEV c2 pfma(c2 a, float k, c2 c) { return __builtin_elementwise_fma(a, c2{k, k}, c); }
DEV c2 ptw(float f) { c2 r = {__builtin_amdgcn_cosf(f), -__builtin_amdgcn_sinf(f)}; PIN(r); return r; }
DEV void pfft4(c2& a0, c2& a1, c2& a2, c2& a3) {
    c2 t0 = a0 + a2, t1 = a0 - a2, t2 = a1 + a3, t3 = a1 - a3;
    a0 = t0 + t2; a2 = t0 - t2; a1 = subi(t1, t3); a3 = addi(t1, t3);
}
DEV void pfft8(c2 (&a)[8]) {
    pfft4(a[0], a[2], a[4], a[6]);
    pfft4(a[1], a[3], a[5], a[7]);
    const c2 e0 = a[0], e1 = a[2], e2 = a[4], e3 = a[6], o0 = a[1], o2 = a[5];
    const c2 t1 = subi(a[3], a[3]), t3 = addi(a[7], a[7]);
    a[0] = e0 + o0; a[4] = e0 - o0;
    a[1] = pfma(t1, RH, e1); a[5] = pfma(t1, -RH, e1);
    a[2] = subi(e2, o2); a[6] = addi(e2, o2);
    a[3] = pfma(t3, -RH, e3); a[7] = pfma(t3, RH, e3);
}
DEV void pfft5(c2 (&a)[5]) {
    constexpr float c1 = 0.30901699437494742f, cc2 = -0.80901699437494742f;
    constexpr float s1 = 0.95105651629515357f, s2 = 0.58778525229247313f;
    const c2 t1 = a[1] + a[4], t2 = a[2] + a[3], t3 = a[1] - a[4], t4 = a[2] - a[3];
    const c2 m1 = pfma(t2, cc2, pfma(t1, c1, a[0])), m2 = pfma(t2, c1, pfma(t1, cc2, a[0]));
    const c2 n1 = pfma(t4, s2, t3 * s1), n2 = pfma(t4, -s1, t3 * s2);
    a[0] = a[0] + t1 + t2;
    a[1] = subi(m1, n1); a[4] = addi(m1, n1);
    a[2] = subi(m2, n2); a[3] = addi(m2, n2);
}
DEV void pfft10(c2 (&a)[10]) {
    c2 e[5], o[5];
#pragma unroll
    for (int n2 = 0; n2 < 5; ++n2) { const c2 p = a[(2 * n2) % 10], q = a[(2 * n2 + 5) % 10]; e[n2] = p + q; o[n2] = p - q; }
    pfft5(e); pfft5(o);
#pragma unroll
    for (int k2 = 0; k2 < 5; ++k2) { a[(6 * k2) % 10] = e[k2]; a[(6 * k2 + 5) % 10] = o[k2]; }
}
DEV c2 pld(const char* base, int idx) { return *(const LAS c2*)((LAS const char*)base + idx * 8); }
DEV void pst(char* base, int idx, c2 a) { *(LAS c2*)((LAS char*)base + idx * 8) = a; }
DEV unsigned pack_f16(c2 a) { return __builtin_bit_cast(unsigned, __builtin_convertvector(a, f16x2)); }

constexpr unsigned CANARY = 0x7f817f81u;
constexpr int BUFA = 4 * 720 * 8, BUFB = 4 * 656 * 8, XP = 1296;

__global__ __launch_bounds__(512, 2) void k1_kernel(const unsigned* __restrict__ x32, const char* __restrict__ w1, const char* __restrict__ w3,
                                                   const unsigned short* __restrict__ wr, const unsigned short* __restrict__ wi, char* __restrict__ ws) {
    __shared__ __attribute__((aligned(16))) char smem[BUFA + BUFB];
    const int b = blockIdx.x, tid = threadIdx.x, lane = tid & 63;
    const int wv = __builtin_amdgcn_readfirstlane(tid >> 6);
    STAMP_DECL;
    STAMP(0, 0);
    if (b < 256) {
        const int s0 = s0_of(b);
        const int s = s0 + 256 * wv;
        unsigned z[5];
#pragma unroll
        for (int i = 0; i < 5; ++i) z[i] = x32[s * 320 + lane + 64 * i];
        const int qr = tid / 80, r = tid - qr * 80;
        c2 t640[8], t80[8];
        if (wv < 5) {
#pragma unroll
            for (int a = 1; a < 8; ++a) t640[a] = ptw((float)(r * a) * (1.0f / 640.0f));
#pragma unroll
            for (int a = 1; a < 8; ++a) t80[a] = ptw((float)((r >> 3) * a) * (1.0f / 80.0f));
        }
        const c2 twq = ptw((float)(s0 * (lane & 7)) * (1.0f / 2048.0f));
        SB();
        float v[10];
#pragma unroll
        for (int i = 0; i < 5; ++i) { v[2 * i] = bf_lo(z[i]); v[2 * i + 1] = bf_hi(z[i]); }
        const float c0 = rdlane(v[0], 0);
        STAMP(0, 1);
        float s1 = 0.f, s2 = 0.f;
#pragma unroll
        for (int i = 0; i < 10; ++i) { float d = v[i] - c0; s1 += d; s2 += d * d; }
        s1 = wave_sum(s1); s2 = wave_sum(s2);
        const float md = s1 * (1.0f / 640.0f);
        float var = s2 * (1.0f / 640.0f) - md * md;
        var = var < 0.f ? 0.f : var;
        const float m = rbf(c0 + md);
        const float sd = rbf(__builtin_amdgcn_sqrtf(rbf(rbf(var) + 1e-5f)));
        const float rs = __builtin_amdgcn_rcpf(sd);
        char* bufA = smem;
        char* bufB = smem + BUFA;
#pragma unroll
        for (int i = 0; i < 5; ++i) {
            unsigned d = pack_bf16(v[2 * i] - m, v[2 * i + 1] - m);
            *(LAS unsigned*)((LAS char*)bufB + wv * XP + (lane + 64 * i) * 4) = pack_bf16(bf_lo(d) * rs, bf_hi(d) * rs);
        }
        wait_lgkm0();
        bar();
        STAMP(0, 2);
        if (wv >= 5) {
            u32x4 pc[4];
#pragma unroll
            for (int i = 0; i < 4; ++i) { const int p = tid - 320 + 192 * i, pp = p < 640 ? p : 0; pc[i] = *(const LAS u32x4*)((LAS const char*)bufB + (pp & 7) * XP + (pp >> 3) * 16); }
            wait_lgkm0();
            bar();
            char* xt = ws + WS_XN + (size_t)(s0 >> 4) * (20 * 8192) + ((s0 >> 2) & 3) * 1024 + (s0 & 3) * 128;
#pragma unroll
            for (int i = 0; i < 4; ++i) {
                const int p = tid - 320 + 192 * i, c = p >> 3;
                if (p < 640) *(u32x4*)(xt + (c >> 1) * 4096 + (c & 1) * 512 + (p & 7) * 16) = pc[i];
            }
            unsigned t1[3], t3[3];
#pragma unroll
            for (int i = 0; i < 3; ++i) {
                const int L = (tid - 320) + 192 * i;
                const size_t off = (size_t)b * 7680 + (L < 480 ? L : 0) * 16;
                t1[i] = *(const unsigned*)(w1 + off); t3[i] = *(const unsigned*)(w3 + off);
            }
            bar(); bar();
            asm volatile("" :: "v"(t1[0]), "v"(t1[1]), "v"(t1[2]), "v"(t3[0]), "v"(t3[1]), "v"(t3[2]));
            STAMP(0, 5); STAMP_FLUSH(0);
            return;
        }
        {
            c2 xr[8];
#pragma unroll
            for (int j = 0; j < 8; ++j) { const unsigned w = *(const LAS unsigned*)((LAS const char*)bufB + j * XP + tid * 4); xr[j] = c2{bf_lo(w), bf_hi(w)}; }
            const c2 a0 = xr[0] + xr[4], a1 = xr[0] - xr[4], b0 = xr[2] + xr[6], b1 = xr[2] - xr[6];
            const c2 e0 = xr[1] + xr[5], e1 = xr[1] - xr[5], d0 = xr[3] + xr[7], d1 = xr[3] - xr[7];
            const c2 p = a0 + b0, q = e0 + d0, u = e1 - d1, w = e1 + d1;
            const c2 y0 = p + q, y4 = p - q, y2r = a0 - b0, y2i = d0 - e0;
            const c2 y1r = pfma(u, RH, a1), y1i = pfma(w, -RH, -b1), y3r = pfma(u, -RH, a1), y3i = pfma(w, -RH, b1);
            *(LAS f32x4*)((LAS char*)bufA + (0 * 720 + 2 * tid) * 8) = f32x4{y0.x, y4.x, y0.y, y4.y};
            *(LAS f32x4*)((LAS char*)bufA + (1 * 720 + 2 * tid) * 8) = f32x4{y1r.x, y1i.x, y1r.y, y1i.y};
            *(LAS f32x4*)((LAS char*)bufA + (2 * 720 + 2 * tid) * 8) = f32x4{y2r.x, y2i.x, y2r.y, y2i.y};
            *(LAS f32x4*)((LAS char*)bufA + (3 * 720 + 2 * tid) * 8) = f32x4{y3r.x, y3i.x, y3r.y, y3i.y};
        }
        wait_lgkm0();
        bar();
        c2 a8[8];
        {
#pragma unroll
            for (int i = 0; i < 8; ++i) a8[i] = pld(bufA, qr * 720 + r + 80 * i);
            pfft8(a8);
            pst(bufB, qr * 656 + r, a8[0]);
#pragma unroll
            for (int a = 1; a < 8; ++a) pst(bufB, qr * 656 + a * 82 + r, pmul(a8[a], t640[a]));
        }
        wait_lgkm0();
        bar();
        STAMP(0, 3);
        {
            const int ib = qr * 656 + (r & 7) * 82 + (r >> 3), ob = qr * 720 + (r >> 3) * 72 + (r & 7);
#pragma unroll
            for (int i = 0; i < 8; ++i) a8[i] = pld(bufB, ib + 10 * i);
            pfft8(a8);
            pst(bufA, ob, a8[0]);
#pragma unroll
            for (int a = 1; a < 8; ++a) pst(bufA, ob + 8 * a, pmul(a8[a], t80[a]));
        }
        wait_lgkm0();
        bar();
        STAMP(0, 4);
        if (wv < 4) {
            c2 f[10];
#pragma unroll
            for (int t = 0; t < 10; ++t) f[t] = pld(bufA, wv * 720 + t * 72 + lane);
            pfft10(f);
            unsigned* T = (unsigned*)(ws + WS_T16) + (size_t)s0 * (8 * TP);
            if (wv == 0) {
#pragma unroll
                for (int bb = 5; bb < 10; ++bb) pst(bufB, lane + 64 * (bb - 5), f[bb]);
                if (lane == 0) pst(bufB, 320, f[0]);
                wave_fence();
                const c2 w4 = {rdlane(twq.y, 4) * 0.5f, rdlane(twq.x, 4) * -0.5f};
#pragma unroll
                for (int bb = 0; bb < 5; ++bb) {
                    const c2 h = pld(bufB, 320 - lane - 64 * bb);
                    T[lane + 64 * bb] = pack_f16(addc(f[bb], h) * 0.5f);
                    T[4 * TP + lane + 64 * bb] = pack_f16(pmul(subc(f[bb], h), w4));
                }
                if (lane == 0) {
                    T[320] = pack_f16(c2{f[5].x, 0.f});
                    T[4 * TP + 320] = pack_f16(pmul(subc(f[5], f[5]), w4));
                }
            } else {
                const c2 wa = {rdlane(twq.x, wv), rdlane(twq.y, wv)}, wb = {rdlane(twq.x, 8 - wv), rdlane(twq.y, 8 - wv)};
                unsigned* Ta = T + wv * TP;
                unsigned* Tb = T + (8 - wv) * TP;
#pragma unroll
                for (int bb = 0; bb < 5; ++bb) Ta[lane + 64 * bb] = pack_f16(pmul(f[bb], wa));
#pragma unroll
                for (int bb = 5; bb < 10; ++bb) Tb[640 - lane - 64 * bb] = pack_f16(pmulc(f[bb], wb));
                if (lane == 0) {
                    Tb[0] = pack_f16(pmulc(f[0], wb));
                    Ta[320] = pack_f16(pmul(f[5], wa));
                }
            }
        }
        STAMP(0, 5); STAMP_FLUSH(0);
    } else {
        const int id = b - 256, q = id & 7, a = id >> 3;
        const int k = tid;
        if (k < 4 * NG4) {
            unsigned o[16];
            if (k < DF_) {
#pragma unroll
                for (int i = 0; i < 16; ++i) {
                    const int u = 8 * (a + 16 * i) + q;
                    o[i] = (unsigned)wr[u * DF_ + k] | ((unsigned)wi[u * DF_ + k] << 16);
                }
            } else {
#pragma unroll
                for (int i = 0; i < 16; ++i) o[i] = 0u;
            }
            u32x4* Wt = (u32x4*)(ws + WS_WT) + (q * NG4 + (k >> 2)) * 256 + a * 4 + (k & 3);
#pragma unroll
            for (int bq = 0; bq < 4; ++bq) Wt[bq * 64] = u32x4{o[4 * bq], o[4 * bq + 1], o[4 * bq + 2], o[4 * bq + 3]};
        }
        STAMP(0, 5); STAMP_FLUSH(0);
    }
}

constexpr int WGRP = 3104, SLOT = 4 * WGRP, NS = 10, KSTEPS = 20;
constexpr int FSCR = 16 * 68 * 8;
constexpr int L2_F = NS * SLOT, L2_FLAG = L2_F + 3 * FSCR;
constexpr int L2_W2 = L2_FLAG + 64;
constexpr int K2_LDS = L2_W2 + 10240;

template <int N> DEV void wait_vm() { asm volatile("s_waitcnt vmcnt(%0)" ::"i"(N) : "memory"); }
DEV unsigned lds_min4(const char* smem, int off) {
    u32x4 c = *(const volatile LAS u32x4*)((LAS const char*)smem + off);
    unsigned a = c.x < c.y ? c.x : c.y, b = c.z < c.w ? c.z : c.w;
    return __builtin_amdgcn_readfirstlane(a < b ? a : b);
}
DEV unsigned lds_word(const char* smem, int off) { return __builtin_amdgcn_readfirstlane(*(const volatile LAS unsigned*)((LAS const char*)smem + off)); }
struct Dma { unsigned wo[3]; const char* w1b; const char* w3b; unsigned lw; char* smem; char* ws; };
constexpr int issued_before(int p) { int n = p == 0 ? 2 : p + 3; return n > KSTEPS ? KSTEPS : n; }
template <int ST> DEV void dma_stage(const Dma& d) {
    if constexpr (ST >= NS) {
        while (lds_min4(d.smem, L2_FLAG + 16) < (unsigned)(ST - NS + 1)) __builtin_amdgcn_s_sleep(1);
    }
#pragma unroll
    for (int ks = 0; ks < 2; ++ks) {
        const char* g1 = d.w1b + (size_t)(2 * ST + ks) * (16 * F_ * 2);
        const char* g3 = d.w3b + (size_t)(2 * ST + ks) * (16 * F_ * 2);
        const unsigned l0 = d.lw + (ST % NS) * SLOT + ks * (2 * WGRP), l1 = l0 + WGRP;
        asm volatile("s_mov_b32 m0, %5\n\ts_nop 0\n\t"
                     "global_load_lds_dwordx4 %0, %3\n\t"
                     "global_load_lds_dwordx4 %1, %3 offset:1024\n\t"
                     "global_load_lds_dwordx4 %2, %3 offset:2048\n\t"
                     "s_mov_b32 m0, %6\n\ts_nop 0\n\t"
                     "global_load_lds_dwordx4 %0, %4\n\t"
                     "global_load_lds_dwordx4 %1, %4 offset:1024\n\t"
                     "global_load_lds_dwordx4 %2, %4 offset:2048"
                     :: "v"(d.wo[0]), "v"(d.wo[1]), "v"(d.wo[2]), "s"(g1), "s"(g3), "s"(l0), "s"(l1) : "memory");
    }
}
template <int A, int B> DEV void dma_range(const Dma& d) { if constexpr (A < B) { dma_stage<A>(d); dma_range<A + 1, B>(d); } }
template <int P> DEV void dma_run(const Dma& d) {
    if constexpr (P < KSTEPS) {
        if constexpr (P > 0) dma_range<issued_before(P - 1), issued_before(P)>(d);
        wait_vm<12 * (issued_before(P) - 1 - P)>();
        *(volatile LAS unsigned*)((LAS char*)d.smem + L2_FLAG) = (unsigned)(P + 1);
        dma_run<P + 1>(d);
    }
}

DEV float silu_f(float v) { return v * __builtin_amdgcn_rcpf(1.0f + __builtin_amdgcn_exp2f(v * -1.44269504088896341f)); }
DEV u32x4 wfrag(const char* smem, unsigned a) {
    s16x4 lo = __builtin_amdgcn_ds_read_tr16_b64_v4i16((LAS s16x4*)((LAS char*)smem + a));
    s16x4 hi = __builtin_amdgcn_ds_read_tr16_b64_v4i16((LAS s16x4*)((LAS char*)smem + a + 768));
    return __builtin_bit_cast(u32x4, __builtin_shufflevector(lo, hi, 0, 1, 2, 3, 4, 5, 6, 7));
}

__global__ NOPK __launch_bounds__(512) void k2_kernel(const char* __restrict__ w1, const char* __restrict__ w3, const char* __restrict__ w2,
                                                       char* __restrict__ ws) {
    extern __shared__ __attribute__((aligned(16))) char smem[];
    const int b = blockIdx.x, tid = threadIdx.x, lane = tid & 63;
    const int wv = __builtin_amdgcn_readfirstlane(tid >> 6);
    const int xcd = b & 7, idx = b >> 3;
    const int mt = (xcd & 3) * 4 + (idx & 3), nt = (xcd >> 2) * 8 + (idx >> 2);
    STAMP_DECL;
    STAMP(1, 0);
    if (wv == 0) {
        __builtin_amdgcn_s_setprio(2);
        Dma d;
#pragma unroll
        for (int r = 0; r < 3; ++r) {
            const int L = r * 64 + lane, row = L / 12, c = L - row * 12;
            d.wo[r] = (unsigned)(row * (F_ * 2) + c * 16 - r * 1024);
        }
        d.smem = smem; d.ws = ws;
        d.w1b = w1 + nt * 192; d.w3b = w3 + nt * 192;
        d.lw = (unsigned)(uintptr_t)(LAS char*)smem;
        if (tid < 16) *(volatile LAS unsigned*)((LAS char*)smem + L2_FLAG + tid * 4) = 0u;
        wait_lgkm0();
        dma_range<0, 1>(d);
        bar();
        dma_range<1, issued_before(0)>(d);
        STAMP(1, 1);
        dma_run<0>(d);
        STAMP(1, 11);
        unsigned wo[5];
#pragma unroll
        for (int r = 0; r < 5; ++r) {
            const int L = r * 64 + lane, row = L / 10, c = L - row * 10;
            wo[r] = (unsigned)(row * (D_ * 2) + c * 16 - (r & 3) * 1024);
        }
        const int nu = b < 128 ? 2 : 1;
        for (int i = 0; i < nu; ++i) {
            const int u = b + 256 * i, nt2 = u / 48, kb = u - nt2 * 48;
            const char* g = w2 + (size_t)kb * (32 * D_ * 2) + nt2 * 160;
            const unsigned l0 = d.lw + L2_W2 + i * 5120, l1 = l0 + 4096;
            asm volatile("s_mov_b32 m0, %6\n\ts_nop 0\n\t"
                         "global_load_lds_dwordx4 %0, %5\n\t"
                         "global_load_lds_dwordx4 %1, %5 offset:1024\n\t"
                         "global_load_lds_dwordx4 %2, %5 offset:2048\n\t"
                         "global_load_lds_dwordx4 %3, %5 offset:3072\n\t"
                         "s_mov_b32 m0, %7\n\ts_nop 0\n\t"
                         "global_load_lds_dwordx4 %4, %5"
                         :: "v"(wo[0]), "v"(wo[1]), "v"(wo[2]), "v"(wo[3]), "v"(wo[4]), "s"(g), "s"(l0), "s"(l1) : "memory");
        }
        char* sp = ws + WS_S + (size_t)b * 10240 + lane * 16;
#pragma unroll
        for (int i = 0; i < 10; ++i) st16_wt(sp + i * 1024, u32x4{CANARY, CANARY, CANARY, CANARY});
        wait_vm<10>();
        const int gq = lane >> 4;
        const unsigned ra = L2_W2 + (16 * (gq >> 1) + 4 * (gq & 1) + ((lane & 15) >> 2)) * 160 + (lane & 3) * 8;
        for (int i = 0; i < nu; ++i) {
            char* dst = ws + WS_W2T + (size_t)(b + 256 * i) * 5120 + lane * 16;
#pragma unroll
            for (int j = 0; j < 5; ++j) {
                s16x4 lo = __builtin_amdgcn_ds_read_tr16_b64_v4i16((LAS s16x4*)((LAS char*)smem + ra + i * 5120 + j * 32));
                s16x4 hi = __builtin_amdgcn_ds_read_tr16_b64_v4i16((LAS s16x4*)((LAS char*)smem + ra + i * 5120 + j * 32 + 8 * 160));
                st16_wt(dst + j * 1024, __builtin_bit_cast(u32x4, __builtin_shufflevector(lo, hi, 0, 1, 2, 3, 4, 5, 6, 7)));
            }
        }
        STAMP(1, 12); STAMP_FLUSH(1);
    } else if (wv >= 4) {
        __builtin_amdgcn_s_setprio(3);
        const int mw = wv - 4;
        const int h = lane >> 5, r32 = lane & 31;
        const int l16 = lane * 16;
        const __amdgpu_buffer_rsrc_t xr = __builtin_amdgcn_make_buffer_rsrc((void*)(ws + WS_XN + (size_t)mt * (20 * 8192) + mw * 1024), 0, 20 * 8192, 0x00020000);
        u32x4 xf[5][2];
#pragma unroll
        for (int st = 0; st < 5; ++st) {
            xf[st][0] = __builtin_amdgcn_raw_buffer_load_b128(xr, l16, st * 8192, 0);
            xf[st][1] = __builtin_amdgcn_raw_buffer_load_b128(xr, l16, st * 8192 + 4096, 0);
            if (st == 0) bar();
        }
        f32x16 acc[6];
#pragma unroll
        for (int i = 0; i < 6; ++i)
#pragma unroll
            for (int r = 0; r < 16; ++r) acc[i][r] = 0.f;
        u32x4 wa[6], wb[6];
        unsigned rc = ((lane >> 4) & 1) * WGRP + (8 * h + ((lane & 15) >> 2)) * 192 + (lane & 3) * 8, rn = rc + 5 * SLOT;
        unsigned fa = L2_FLAG, da = L2_FLAG + 16 + mw * 4, dv = 0;
        PIN(rc); PIN(rn); PIN(fa); PIN(da); PIN(dv);
#define MM(i, w, x) asm volatile("v_mfma_f32_32x32x16_bf16 %0, %1, %2, %0" : "+v"(acc[i]) : "v"(w), "v"(x) : "memory")
        while (lds_word(smem, L2_FLAG) < 1u) __builtin_amdgcn_s_sleep(1);
        STAMP(1, 1);
#pragma unroll
        for (int i = 0; i < 6; ++i) wa[i] = wfrag(smem, rc + i * 32);
#pragma unroll 1
        for (int io = 0; io < 3; ++io) {
#pragma unroll
            for (int ii = 0; ii < 5; ++ii) {
                const int it = io * 5 + ii;
                const unsigned cb = rc + ii * SLOT + 2 * WGRP, nb = ii < 4 ? rc + (ii + 1) * SLOT : rn;
                MM(0, wa[0], xf[ii][0]);
                wb[0] = wfrag(smem, cb);
                unsigned fl = *(const volatile LAS unsigned*)((LAS const char*)smem + fa);
                MM(1, wa[1], xf[ii][0]);
                wb[1] = wfrag(smem, cb + 32);
                MM(2, wa[2], xf[ii][0]);
                wb[2] = wfrag(smem, cb + 64);
                MM(3, wa[3], xf[ii][0]);
                wb[3] = wfrag(smem, cb + 96);
                asm volatile("v_add_u32 %0, 1, %0" : "+v"(dv));
                MM(4, wa[4], xf[ii][0]);
                wb[4] = wfrag(smem, cb + 128);
                {
                    unsigned m = __builtin_amdgcn_readfirstlane(fl);
                    const unsigned need = it == 14 ? KSTEPS : it + 2;
                    while (m < need) { __builtin_amdgcn_s_sleep(1); m = lds_word(smem, L2_FLAG); }
                }
                MM(5, wa[5], xf[ii][0]);
                wb[5] = wfrag(smem, cb + 160);
                MM(0, wb[0], xf[ii][1]);
                wa[0] = wfrag(smem, nb);
                xf[ii][0] = __builtin_amdgcn_raw_buffer_load_b128(xr, l16, (it + 5) * 8192, 0);
                MM(1, wb[1], xf[ii][1]);
                wa[1] = wfrag(smem, nb + 32);
                MM(2, wb[2], xf[ii][1]);
                wa[2] = wfrag(smem, nb + 64);
                MM(3, wb[3], xf[ii][1]);
                wa[3] = wfrag(smem, nb + 96);
                MM(4, wb[4], xf[ii][1]);
                wa[4] = wfrag(smem, nb + 128);
                MM(5, wb[5], xf[ii][1]);
                wa[5] = wfrag(smem, nb + 160);
                if ((it & 1) && io < 2) *(volatile LAS unsigned*)((LAS char*)smem + da) = dv;
                xf[ii][1] = __builtin_amdgcn_raw_buffer_load_b128(xr, l16, (it + 5) * 8192 + 4096, 0);
            }
            { const unsigned t = rc; rc = rn; rn = t; }
            STAMP(1, 2 + io); CSTAMP(1, 2 + io);
        }
        char* ht = ws + WS_HT + (size_t)((((mt * 2 + (mw >> 1)) * 48 + 3 * nt) * 4 + 2 * (mw & 1) + (r32 >> 4)) * 64 + (r32 & 15) + 16 * h) * 16;
        u32x4 g0[9], g1[9];
#define TFRAG(i, n) wfrag(smem, rc + ((n) >> 1) * SLOT + ((n) & 1) * (2 * WGRP) + (i) * 32)
#pragma unroll
        for (int n = 1; n < 10; ++n) g0[n - 1] = TFRAG(0, n);
#pragma unroll
        for (int i = 0; i < 7; ++i) {
            float hh[8];
#pragma unroll
            for (int n = 0; n < 10; ++n) {
                if (i < 6) {
                    if (n == 0) MM(i, wa[i], xf[0][0]);
                    else if (i & 1) MM(i, g1[n - 1], xf[n >> 1][n & 1]);
                    else MM(i, g0[n - 1], xf[n >> 1][n & 1]);
                    if (i < 5 && n < 9) { if (i & 1) g0[n] = TFRAG(i + 1, n + 1); else g1[n] = TFRAG(i + 1, n + 1); }
                } else if (n == 0) {
                    asm volatile("s_nop 15\n\ts_nop 7" ::: "memory");
                }
                if (i > 0) {
                    if (n >= 1 && n <= 8) { hh[n - 1] = silu_f(acc[i - 1][n - 1]) * acc[i - 1][8 + n - 1]; PIN(hh[n - 1]); }
                    if (n == 9) {
                        const u32x4 o = {pack_bf16(hh[0], hh[1]), pack_bf16(hh[2], hh[3]), pack_bf16(hh[4], hh[5]), pack_bf16(hh[6], hh[7])};
                        *(u32x4*)(ht + ((i - 1) >> 1) * 4096 + ((i - 1) & 1) * 512) = o;
                    }
                }
                SB();
            }
        }
        STAMP(1, 12); STAMP_FLUSH(1);
    } else {
        const int fw = wv - 1;
        const int q = xcd, g4 = fw * 32 + idx;
        if (g4 >= NG4) return;
        bar();
        const int t = lane >> 2;
        cf tw[16];
#pragma unroll
        for (int a = 1; a < 16; ++a) { tw[a] = twid((float)(t * a) * (1.0f / 256.0f)); PIN(tw[a].x); PIN(tw[a].y); }
        SB();
        while (lds_word(smem, L2_FLAG) < 4u) __builtin_amdgcn_s_sleep(2);
        const int eo = (t * 8 + q) * TP + 4 * g4 + (lane & 3);
        const __amdgpu_buffer_rsrc_t tr = __builtin_amdgcn_make_buffer_rsrc((void*)(ws + WS_T16), 0, 2048 * TP * 4, 0x00020000);
        const __amdgpu_buffer_rsrc_t vr = __builtin_amdgcn_make_buffer_rsrc((void*)(ws + WS_T), 0, 2048 * TP * 8, 0x00020000);
        unsigned tv[16];
        u32x4 wq[4];
#pragma unroll
        for (int i = 0; i < 16; ++i) tv[i] = __builtin_amdgcn_raw_buffer_load_b32(tr, eo * 4, i * (128 * TP * 4), 0);
#pragma unroll
        for (int i = 0; i < 4; ++i) wq[i] = ((const u32x4*)(ws + WS_WT))[(size_t)(q * NG4 + g4) * 256 + i * 64 + lane];
        SB();
        char* fs = smem + L2_F + fw * FSCR;
        cf v[16];
#pragma unroll
        for (int i = 0; i < 16; ++i) { const f16x2 hv = __builtin_bit_cast(f16x2, tv[i]); v[i] = {(float)hv.x, (float)hv.y}; }
        STAMP(1, 1);
        fft16_a<-1>(v); fft16_t<-1>(v); fft16_b<-1>(v);
        lds_st(fs, lane, v[0]);
#pragma unroll
        for (int a = 1; a < 16; ++a) lds_st(fs, a * 68 + lane, cmul(v[a], tw[a]));
        wave_fence();
#pragma unroll
        for (int i = 0; i < 16; ++i) v[i] = lds_ld(fs, t * 68 + i * 4 + (lane & 3));
        fft16_a<-1>(v); fft16_t<-1>(v); fft16_b<-1>(v);
#pragma unroll
        for (int i = 0; i < 16; ++i) { const unsigned w = wq[i >> 2][i & 3]; v[i] = cmul(v[i], cf{bf_lo(w), bf_hi(w)}); }
        fft16_a<1>(v); fft16_t<1>(v); fft16_b<1>(v);
        wave_fence();
        lds_st(fs, t * 68 + (lane & 3), v[0]);
#pragma unroll
        for (int i = 1; i < 16; ++i) lds_st(fs, t * 68 + i * 4 + (lane & 3), cmulc(v[i], tw[i]));
        wave_fence();
#pragma unroll
        for (int a = 0; a < 16; ++a) v[a] = lds_ld(fs, a * 68 + lane);
        fft16_a<1>(v); fft16_t<1>(v); fft16_b<1>(v);
#pragma unroll
        for (int i = 0; i < 16; ++i)
            __builtin_amdgcn_raw_buffer_store_b64(__builtin_bit_cast(u32x2, f32x2{v[i].x, v[i].y}), vr, eo * 8, i * (128 * TP * 8), 0);
        STAMP(1, 12); STAMP_FLUSH(1);
    }
}

constexpr int RPITCH = 336;
constexpr int L3_RED = 0;
constexpr int L3_A = 4 * 64 * RPITCH;
constexpr int L3_B = L3_A + 4 * 720 * 8;
constexpr int L3_CNT = L3_B + 4 * 656 * 8;
constexpr int K3_LDS = L3_CNT + 64;
#define GO_STEP 9

DEV void helper_barrier(char* smem, int lane, unsigned target) {
    wait_lgkm0();
    LAS unsigned* cnt = (LAS unsigned*)((LAS char*)smem + L3_CNT);
    if (lane == 0) __hip_atomic_fetch_add(cnt, 1u, __ATOMIC_RELAXED, __HIP_MEMORY_SCOPE_WORKGROUP);
    while (__hip_atomic_load(cnt, __ATOMIC_RELAXED, __HIP_MEMORY_SCOPE_WORKGROUP) < target) {}
    asm volatile("" ::: "memory");
}
#define MFMA16(a, b, c) __builtin_amdgcn_mfma_f32_16x16x32_bf16(__builtin_bit_cast(bf16x8, a), __builtin_bit_cast(bf16x8, b), c, 0, 0, 0)

__global__ NOPK __launch_bounds__(768) void k3_kernel(const char* __restrict__ x, char* __restrict__ ws, char* __restrict__ out) {
    extern __shared__ __attribute__((aligned(16))) char smem[];
    const int b = blockIdx.x, tid = threadIdx.x, lane = tid & 63;
    const int wv = __builtin_amdgcn_readfirstlane(tid >> 6);
    const int xcd = b & 7, idx = b >> 3;
    const int mt2 = (xcd & 3) * 8 + (idx & 7), nt2 = (xcd >> 2) * 4 + (idx >> 3);
    STAMP_DECL;
    STAMP(2, 0);
    if (tid == 0) *(LAS u32x2*)((LAS char*)smem + L3_CNT) = u32x2{0u, 0u};
    wait_lgkm0();
    bar();
    if (wv >= 8) {
        const int gw = wv - 8;
        const char* hb = ws + WS_HT + (size_t)(mt2 * 48 + 12 * gw) * 4096;
        const char* wb = ws + WS_W2T + (size_t)(nt2 * 48 + 12 * gw) * 5120;
        const int l16 = lane * 16;
        const __amdgpu_buffer_rsrc_t hr = __builtin_amdgcn_make_buffer_rsrc((void*)hb, 0, 12 * 4096, 0x00020000);
        const __amdgpu_buffer_rsrc_t wr_ = __builtin_amdgcn_make_buffer_rsrc((void*)wb, 0, 12 * 5120, 0x00020000);
#define HLD(st, tb) __builtin_amdgcn_raw_buffer_load_b128(hr, l16 + (tb) * 1024, (st) * 4096, 0)
#define WLD(st, j) __builtin_amdgcn_raw_buffer_load_b128(wr_, l16 + ((j) & 3) * 1024, (st) * 5120 + ((j) >> 2) * 4096, 0)
        u32x4 hf[2][4], wf[2][5];
#pragma unroll
        for (int st = 0; st < 2; ++st) {
#pragma unroll
            for (int tb = 0; tb < 4; ++tb) hf[st][tb] = HLD(st, tb);
#pragma unroll
            for (int j = 0; j < 5; ++j) wf[st][j] = WLD(st, j);
            SB();
        }
        f32x4 acc[5][4];
#pragma unroll
        for (int j = 0; j < 5; ++j)
#pragma unroll
            for (int tb = 0; tb < 4; ++tb) acc[j][tb] = f32x4{0.f, 0.f, 0.f, 0.f};
        char* rp = smem + L3_RED + gw * (64 * RPITCH) + (lane & 15) * RPITCH + (lane >> 4) * 16;
        SB();
#pragma unroll
        for (int st = 0; st < 12; ++st) {
            const int sl = st & 1;
            const bool more = st + 2 < 12;
#pragma unroll
            for (int j = 0; j < 4; ++j) {
#pragma unroll
                for (int tb = 0; tb < 4; ++tb) acc[j][tb] = MFMA16(wf[sl][j], hf[sl][tb], acc[j][tb]);
                SB();
                if (more) wf[sl][j] = WLD(st + 2, j);
                if (st == 11 && j > 0) {
#pragma unroll
                    for (int tb = 0; tb < 4; ++tb) *(LAS f32x4*)((LAS char*)rp + tb * (16 * RPITCH) + (j - 1) * 64) = acc[j - 1][tb];
                }
                SB();
            }
#pragma unroll
            for (int tb = 0; tb < 4; ++tb) {
                acc[4][tb] = MFMA16(wf[sl][4], hf[sl][tb], acc[4][tb]);
                SB();
                if (more) hf[sl][tb] = HLD(st + 2, tb);
                SB();
            }
            if (more) wf[sl][4] = WLD(st + 2, 4);
            if (st == GO_STEP) *(volatile LAS unsigned*)((LAS char*)smem + L3_CNT + 4) = 1u;
            SB();
        }
        STAMP(2, 1);
#pragma unroll
        for (int j = 3; j < 5; ++j)
#pragma unroll
            for (int tb = 0; tb < 4; ++tb) *(LAS f32x4*)((LAS char*)rp + tb * (16 * RPITCH) + j * 64) = acc[j][tb];
        wait_lgkm0();
        STAMP(2, 2);
        bar();
        STAMP_FLUSH(2);
    } else {
        __builtin_amdgcn_s_setprio(2);
        const int s0 = s0_of(b);
        const f32x2* V = (const f32x2*)(ws + WS_T) + (size_t)s0 * (8 * TP);
        char* sg = ws + WS_S;
        f32x2 u[10], u2[10], e0, e1;
        if (wv < 4) {
            const int qa = wv, qb = wv == 0 ? 0 : 8 - wv;
#pragma unroll
            for (int bb = 0; bb < 5; ++bb) u[bb] = V[qa * TP + lane + 64 * bb];
#pragma unroll
            for (int bb = 5; bb < 10; ++bb) u[bb] = V[qb * TP + 640 - lane - 64 * bb];
            if (wv == 0) {
#pragma unroll
                for (int bb = 0; bb < 5; ++bb) u2[bb] = V[4 * TP + lane + 64 * bb];
#pragma unroll
                for (int bb = 5; bb < 10; ++bb) u2[bb] = V[4 * TP + 640 - lane - 64 * bb];
            } else {
                e0 = V[qb * TP];
                e1 = V[qa * TP + 320];
            }
        }
        const int pc0 = tid, pc1 = wv >= 6 ? tid + 128 : tid;
        const int r0 = pc0 / 10, c0 = pc0 - r0 * 10, r1 = pc1 / 10, c1 = pc1 - r1 * 10;
        const size_t eo0 = (size_t)(mt2 * 8 + (r0 >> 3) + 256 * (r0 & 7)) * (D_ * 2) + nt2 * 160 + c0 * 16;
        const size_t eo1 = (size_t)(mt2 * 8 + (r1 >> 3) + 256 * (r1 & 7)) * (D_ * 2) + nt2 * 160 + c1 * 16;
        const u32x4 xa0 = *(const u32x4*)(x + eo0);
        const u32x4 xa1 = *(const u32x4*)(x + eo1);
        STAMP(2, 12);
        if (wv < 5) {
            constexpr float NORM = 1.0f / (2048.0f * 640.0f);
            const int qr = tid / 80, r = tid - qr * 80;
            cf t80[10], t640[8];
#pragma unroll
            for (int t = 1; t < 10; ++t) { t80[t] = twid((float)((lane >> 3) * t) * (1.0f / 80.0f)); PIN(t80[t].x); PIN(t80[t].y); }
#pragma unroll
            for (int a = 1; a < 8; ++a) { t640[a] = twid((float)(r * a) * (1.0f / 640.0f)); PIN(t640[a].x); PIN(t640[a].y); }
            cf twq = twid((float)(s0 * (lane & 7)) * (1.0f / 2048.0f));
            SB();
            STAMP(2, 11);
            char* bufA = smem + L3_A;
            char* bufB = smem + L3_B;
            if (wv < 4) {
                cf f[10];
                if (wv == 0) {
                    const cf w4 = {rdlane(twq.x, 4) * NORM, rdlane(twq.y, 4) * NORM};
#pragma unroll
                    for (int bb = 0; bb < 10; ++bb) {
                        cf A = {u[bb].x * NORM, u[bb].y * NORM}, B = cmulc(cf{u2[bb].x, u2[bb].y}, w4);
                        if (bb == 0 || bb == 5) { if (lane == 0) { A.y = 0.f; B.y = 0.f; } }
                        f[bb] = bb < 5 ? cf{A.x - B.y, A.y + B.x} : cf{A.x + B.y, B.x - A.y};
                    }
                } else {
                    const float N2 = 2.0f * NORM;
                    const cf wa = {rdlane(twq.x, wv) * N2, rdlane(twq.y, wv) * N2}, wb = {rdlane(twq.x, 8 - wv) * N2, rdlane(twq.y, 8 - wv) * N2};
#pragma unroll
                    for (int bb = 0; bb < 5; ++bb) f[bb] = cmulc(cf{u[bb].x, u[bb].y}, wa);
#pragma unroll
                    for (int bb = 5; bb < 10; ++bb) { cf t = cmulc(cf{u[bb].x, u[bb].y}, wb); f[bb] = cf{t.x, -t.y}; }
                    const cf g0 = cmulc(cf{e0.x, e0.y}, wb), g1 = cmulc(cf{e1.x, e1.y}, wa);
                    if (lane == 0) {
                        f[0] = cf{0.5f * (f[0].x + g0.x), 0.5f * (f[0].y - g0.y)};
                        f[5] = cf{0.5f * (f[5].x + g1.x), 0.5f * (f[5].y + g1.y)};
                    }
                }
                STAMP(2, 1);
                fft10<1>(f);
                lds_st(bufA, wv * 720 + lane, f[0]);
#pragma unroll
                for (int t = 1; t < 10; ++t) lds_st(bufA, wv * 720 + t * 72 + lane, cmulc(f[t], t80[t]));
            }
            STAMP(2, 2);
            helper_barrier(smem, lane, 5);
            STAMP(2, 13);
            cf a8[8];
            {
                const int base = qr * 720 + (r >> 3) * 72 + (r & 7);
#pragma unroll
                for (int i = 0; i < 8; ++i) a8[i] = lds_ld(bufA, base + 8 * i);
                fft8<1>(a8);
                const int ob = qr * 656 + (r & 7) * 82 + (r >> 3);
#pragma unroll
                for (int i = 0; i < 8; ++i) lds_st(bufB, ob + 10 * i, a8[i]);
            }
            STAMP(2, 14);
            helper_barrier(smem, lane, 10);
            STAMP(2, 15);
            {
                a8[0] = lds_ld(bufB, qr * 656 + r);
#pragma unroll
                for (int a = 1; a < 8; ++a) a8[a] = cmulc(lds_ld(bufB, qr * 656 + a * 82 + r), t640[a]);
                fft8<1>(a8);
#pragma unroll
                for (int i = 0; i < 8; ++i) lds_st(bufA, qr * 720 + r + 80 * i, a8[i]);
            }
            helper_barrier(smem, lane, 15);
            STAMP(2, 3);
            {
                f32x4 p[4];
#pragma unroll
                for (int q = 0; q < 4; ++q) p[q] = *(const LAS f32x4*)((LAS const char*)bufA + (q * 720 + 2 * tid) * 8);
                float y[2][8];
#pragma unroll
                for (int e = 0; e < 2; ++e) {
                    const float P0 = p[0][2 * e], P4 = p[0][2 * e + 1];
                    const float A = P0 + P4, B = P0 - P4;
                    const float c0 = A + p[2][2 * e], c2 = A - p[2][2 * e], c1 = B - p[2][2 * e + 1], c3 = B + p[2][2 * e + 1];
                    const float o0 = p[1][2 * e] + p[3][2 * e], o2 = p[3][2 * e + 1] - p[1][2 * e + 1];
                    const float uu = p[1][2 * e] - p[3][2 * e], vv = p[1][2 * e + 1] + p[3][2 * e + 1];
                    const float o1 = uu - vv, o3 = -uu - vv;
                    y[e][0] = c0 + o0; y[e][4] = c0 - o0;
                    y[e][2] = c2 + o2; y[e][6] = c2 - o2;
                    y[e][1] = c1 + RH * o1; y[e][5] = c1 - RH * o1;
                    y[e][3] = c3 + RH * o3; y[e][7] = c3 - RH * o3;
                }
                char* stg = bufB + wv * 2048;
#pragma unroll
                for (int j = 0; j < 8; ++j) *(LAS unsigned*)((LAS char*)stg + j * 256 + lane * 4) = pack_bf16(y[0][j], y[1][j]);
                wave_fence();
#pragma unroll
                for (int hh = 0; hh < 2; ++hh) {
                    const int j = 4 * hh + (lane >> 4);
                    u32x4 o = *(const LAS u32x4*)((LAS const char*)stg + hh * 1024 + lane * 16);
                    st16_wt(sg + (size_t)(s0 + 256 * j) * (D_ * 2) + wv * 256 + (lane & 15) * 16, o);
                }
            }
            STAMP(2, 4);
        }
        while (lds_word(smem, L3_CNT + 4) == 0u) __builtin_amdgcn_s_sleep(4);
        STAMP(2, 7);
        u32x4 sa0, sa1;
        {
            const char* src0 = sg + eo0;
            const char* src1 = sg + eo1;
            for (unsigned spin = 0; spin < (1u << 20); ++spin) {
                asm volatile("global_load_dwordx4 %0, %2, off sc1\n\tglobal_load_dwordx4 %1, %3, off sc1\n\ts_waitcnt vmcnt(0)"
                             : "=&v"(sa0), "=&v"(sa1) : "v"(src0), "v"(src1) : "memory");
                const bool bad = sa0.x == CANARY || sa0.y == CANARY || sa0.z == CANARY || sa0.w == CANARY ||
                                 sa1.x == CANARY || sa1.y == CANARY || sa1.z == CANARY || sa1.w == CANARY;
                if (__builtin_amdgcn_ballot_w64(bad) == 0ull) break;
                __builtin_amdgcn_s_sleep(8);
            }
        }
        STAMP(2, 8);
        bar();
        STAMP(2, 9);
        for (int rep = 0; rep < (wv >= 6 ? 2 : 1); ++rep) {
            const char* rp = smem + L3_RED + (rep ? r1 : r0) * RPITCH + (rep ? c1 : c0) * 32;
            f32x4 o0 = *(const LAS f32x4*)((LAS const char*)rp), o1 = *(const LAS f32x4*)((LAS const char*)rp + 16);
#pragma unroll
            for (int w = 1; w < 4; ++w) {
                o0 += *(const LAS f32x4*)((LAS const char*)rp + w * (64 * RPITCH));
                o1 += *(const LAS f32x4*)((LAS const char*)rp + w * (64 * RPITCH) + 16);
            }
            const u32x4 sv = rep ? sa1 : sa0, xv = rep ? xa1 : xa0;
            const float o[8] = {o0.x, o0.y, o0.z, o0.w, o1.x, o1.y, o1.z, o1.w};
            u32x4 res;
#pragma unroll
            for (int i = 0; i < 4; ++i)
                res[i] = pack_bf16(bf_lo(xv[i]) + bf_lo(sv[i]) * silu_f(o[2 * i]), bf_hi(xv[i]) + bf_hi(sv[i]) * silu_f(o[2 * i + 1]));
            *(u32x4*)(out + (rep ? eo1 : eo0)) = res;
        }
        STAMP(2, 10); STAMP_FLUSH(2);
    }
}

extern "C" void kernel_launch(void* const* d_in, const int* in_sizes, int n_in, void* d_out, int out_size, void* d_ws,
                              size_t ws_size, hipStream_t stream) {
    (void)in_sizes; (void)n_in; (void)out_size; (void)ws_size;
    char* ws = (char*)d_ws;
    (void)hipFuncSetAttribute((const void*)k2_kernel, hipFuncAttributeMaxDynamicSharedMemorySize, K2_LDS);
    (void)hipFuncSetAttribute((const void*)k3_kernel, hipFuncAttributeMaxDynamicSharedMemorySize, K3_LDS);
    k1_kernel<<<384, 512, 0, stream>>>((const unsigned*)d_in[0], (const char*)d_in[4], (const char*)d_in[5], (const unsigned short*)d_in[1],
                                       (const unsigned short*)d_in[2], ws);
    k2_kernel<<<256, 512, K2_LDS, stream>>>((const char*)d_in[4], (const char*)d_in[5], (const char*)d_in[6], ws);
    k3_kernel<<<256, 768, K3_LDS, stream>>>((const char*)d_in[0], ws, (char*)d_out);
}
```

```cpp
#include <hip/hip_runtime.h>
#include <hip/hip_bf16.h>
#include <stdint.h>
#define TOUCH_W2

typedef float f32x2 __attribute__((ext_vector_type(2)));
typedef float f32x4 __attribute__((ext_vector_type(4)));
typedef float f32x16 __attribute__((ext_vector_type(16)));
typedef short s16x4 __attribute__((ext_vector_type(4)));
typedef short s16x8 __attribute__((ext_vector_type(8)));
typedef __bf16 bf16x2 __attribute__((ext_vector_type(2)));
typedef __bf16 bf16x8 __attribute__((ext_vector_type(8)));
typedef _Float16 f16x2 __attribute__((ext_vector_type(2)));
typedef unsigned u32x2 __attribute__((ext_vector_type(2)));
typedef unsigned u32x4 __attribute__((ext_vector_type(4)));

#define DEV __device__ __forceinline__
#define LAS __attribute__((address_space(3)))
#define GAS __attribute__((address_space(1)))
#if defined(__HIP_DEVICE_COMPILE__)
#define NOPK __attribute__((target("no-packed-fp32-ops")))
#else
#define NOPK
#endif

constexpr int S_ = 2048, D_ = 640, F_ = 1536, DF_ = 321;
constexpr int NG4 = 81;
constexpr int NUNIT = 8 * NG4;

constexpr size_t WS_XN = 0;
constexpr size_t WS_T = 4u << 20;
constexpr size_t WS_WT = 12u << 20;
constexpr size_t WS_W2T = 16u << 20;
constexpr size_t WS_HT = 20u << 20;
constexpr size_t WS_S = 28u << 20;
constexpr size_t WS_T16 = 32u << 20;
constexpr size_t WS_WMAX = 36u << 20;
constexpr int TP = 328;

constexpr size_t WS_DBG = 64u << 20;
#define STAMP_DECL
#define STAMP(kern, id) do {} while (0)
#define CSTAMP(kern, id) do {} while (0)
#define STAMP_FLUSH(kern) do {} while (0)

struct cf { float x, y; };
DEV cf operator+(cf a, cf b) { return {a.x + b.x, a.y + b.y}; }
DEV cf operator-(cf a, cf b) { return {a.x - b.x, a.y - b.y}; }
DEV cf cmul(cf a, cf w) { return {a.x * w.x - a.y * w.y, a.x * w.y + a.y * w.x}; }
DEV cf cmulc(cf a, cf w) { return {a.x * w.x + a.y * w.y, a.y * w.x - a.x * w.y}; }
template <int DIR> DEV cf muli(cf a) { return DIR > 0 ? cf{-a.y, a.x} : cf{a.y, -a.x}; }
DEV cf twid(float f) { return {__builtin_amdgcn_cosf(f), -__builtin_amdgcn_sinf(f)}; }

DEV unsigned pack_bf16(float a, float b) {
    f32x2 v = {a, b};
    return __builtin_bit_cast(unsigned, __builtin_convertvector(v, bf16x2));
}
DEV float bf_lo(unsigned u) { return __uint_as_float(u << 16); }
DEV float bf_hi(unsigned u) { return __uint_as_float(u & 0xffff0000u); }
DEV float rbf(float a) { return bf_lo(pack_bf16(a, a)); }

template <int DIR> DEV void fft4(cf& a0, cf& a1, cf& a2, cf& a3) {
    cf t0 = a0 + a2, t1 = a0 - a2, t2 = a1 + a3, t3 = muli<DIR>(a1 - a3);
    a0 = t0 + t2; a2 = t0 - t2; a1 = t1 + t3; a3 = t1 - t3;
}
constexpr float RH = 0.70710678118654752f;
template <int DIR> DEV cf mulw8(cf a) { return DIR > 0 ? cf{(a.x - a.y) * RH, (a.x + a.y) * RH} : cf{(a.x + a.y) * RH, (a.y - a.x) * RH}; }
template <int DIR> DEV cf mulw83(cf a) { return DIR > 0 ? cf{-(a.x + a.y) * RH, (a.x - a.y) * RH} : cf{(a.y - a.x) * RH, -(a.x + a.y) * RH}; }

template <int DIR> DEV void fft8(cf (&a)[8]) {
    fft4<DIR>(a[0], a[2], a[4], a[6]);
    fft4<DIR>(a[1], a[3], a[5], a[7]);
    cf o1 = mulw8<DIR>(a[3]), o2 = muli<DIR>(a[5]), o3 = mulw83<DIR>(a[7]);
    cf e0 = a[0], e1 = a[2], e2 = a[4], e3 = a[6], o0 = a[1];
    a[0] = e0 + o0; a[4] = e0 - o0;
    a[1] = e1 + o1; a[5] = e1 - o1;
    a[2] = e2 + o2; a[6] = e2 - o2;
    a[3] = e3 + o3; a[7] = e3 - o3;
}
template <int DIR> DEV void fft5(cf (&a)[5]) {
    constexpr float c1 = 0.30901699437494742f, c2 = -0.80901699437494742f;
    constexpr float s1 = 0.95105651629515357f, s2 = 0.58778525229247313f;
    cf t1 = a[1] + a[4], t2 = a[2] + a[3], t3 = a[1] - a[4], t4 = a[2] - a[3];
    cf m1 = {a[0].x + c1 * t1.x + c2 * t2.x, a[0].y + c1 * t1.y + c2 * t2.y};
    cf m2 = {a[0].x + c2 * t1.x + c1 * t2.x, a[0].y + c2 * t1.y + c1 * t2.y};
    cf n1 = muli<DIR>(cf{s1 * t3.x + s2 * t4.x, s1 * t3.y + s2 * t4.y});
    cf n2 = muli<DIR>(cf{s2 * t3.x - s1 * t4.x, s2 * t3.y - s1 * t4.y});
    a[0] = a[0] + t1 + t2;
    a[1] = m1 + n1; a[4] = m1 - n1;
    a[2] = m2 + n2; a[3] = m2 - n2;
}
template <int DIR> DEV void fft10(cf (&a)[10]) {
    cf e[5], o[5];
#pragma unroll
    for (int n2 = 0; n2 < 5; ++n2) { const cf p = a[(2 * n2) % 10], q = a[(2 * n2 + 5) % 10]; e[n2] = p + q; o[n2] = p - q; }
    fft5<DIR>(e); fft5<DIR>(o);
#pragma unroll
    for (int k2 = 0; k2 < 5; ++k2) { a[(6 * k2) % 10] = e[k2]; a[(6 * k2 + 5) % 10] = o[k2]; }
}
template <int DIR> DEV void fft16_a(cf (&a)[16]) {
#pragma unroll
    for (int n2 = 0; n2 < 4; ++n2) fft4<DIR>(a[n2], a[n2 + 4], a[n2 + 8], a[n2 + 12]);
}
template <int DIR> DEV void fft16_t(cf (&a)[16]) {
    constexpr float C1 = 0.92387953251128674f, S1 = 0.38268343236508977f;
    const cf w1 = {C1, DIR * S1}, w3 = {S1, DIR * C1}, w9 = {-C1, -DIR * S1};
    a[1 + 4] = cmul(a[1 + 4], w1);
    a[1 + 8] = mulw8<DIR>(a[1 + 8]);
    a[1 + 12] = cmul(a[1 + 12], w3);
    a[2 + 4] = mulw8<DIR>(a[2 + 4]);
    a[2 + 8] = muli<DIR>(a[2 + 8]);
    a[2 + 12] = mulw83<DIR>(a[2 + 12]);
    a[3 + 4] = cmul(a[3 + 4], w3);
    a[3 + 8] = mulw83<DIR>(a[3 + 8]);
    a[3 + 12] = cmul(a[3 + 12], w9);
}
template <int DIR> DEV void fft16_b(cf (&a)[16]) {
#pragma unroll
    for (int k1 = 0; k1 < 4; ++k1) fft4<DIR>(a[4 * k1], a[4 * k1 + 1], a[4 * k1 + 2], a[4 * k1 + 3]);
    cf b[16];
#pragma unroll
    for (int k1 = 0; k1 < 4; ++k1)
#pragma unroll
        for (int k2 = 0; k2 < 4; ++k2) b[k1 + 4 * k2] = a[4 * k1 + k2];
#pragma unroll
    for (int i = 0; i < 16; ++i) a[i] = b[i];
}

template <int CTRL, int RM> DEV float dpp_add(float v) {
    int t = __builtin_amdgcn_update_dpp(0, __float_as_int(v), CTRL, RM, 0xf, true);
    return v + __int_as_float(t);
}
DEV float wave_sum(float v) {
    v = dpp_add<0x111, 0xf>(v);
    v = dpp_add<0x112, 0xf>(v);
    v = dpp_add<0x114, 0xf>(v);
    v = dpp_add<0x118, 0xf>(v);
    v = dpp_add<0x142, 0xa>(v);
    v = dpp_add<0x143, 0xc>(v);
    return __int_as_float(__builtin_amdgcn_readlane(__float_as_int(v), 63));
}
template <int CTRL, int RM> DEV unsigned dpp_max(unsigned v) {
    const unsigned t = (unsigned)__builtin_amdgcn_update_dpp(0, (int)v, CTRL, RM, 0xf, true);
    return v > t ? v : t;
}
DEV unsigned wave_max(unsigned v) {
    v = dpp_max<0x111, 0xf>(v); v = dpp_max<0x112, 0xf>(v); v = dpp_max<0x114, 0xf>(v); v = dpp_max<0x118, 0xf>(v);
    v = dpp_max<0x142, 0xa>(v); v = dpp_max<0x143, 0xc>(v);
    return (unsigned)__builtin_amdgcn_readlane((int)v, 63);
}
struct WMax { u32x4 a, b, c; };
DEV WMax vscale_load(const char* ws, int lane) { const u32x4* t = (const u32x4*)(ws + WS_WMAX) + lane; return {t[0], t[64], t[128]}; }
DEV int vscale_exp(const WMax& w) {
    const u32x4 a = w.a, b = w.b, c = w.c;
    unsigned m = a.x;
#pragma unroll
    for (int i = 0; i < 4; ++i) { m = m > a[i] ? m : a[i]; m = m > b[i] ? m : b[i]; m = m > c[i] ? m : c[i]; }
    int e = (int)(wave_max(m) >> 7) + 10;
    e = e < 16 ? 16 : e;
    return e > 238 ? 238 : e;
}
DEV float rdlane(float v, int l) { return __int_as_float(__builtin_amdgcn_readlane(__float_as_int(v), l)); }
#define PIN(x) asm volatile("" : "+v"(x))
#define SB() __builtin_amdgcn_sched_barrier(0)
DEV void bar() { SB(); __builtin_amdgcn_s_barrier(); asm volatile("" ::: "memory"); SB(); }
DEV void wait_lgkm0() { asm volatile("s_waitcnt lgkmcnt(0)" ::: "memory"); }
DEV void st16_wt(void* p, u32x4 v) { asm volatile("global_store_dwordx4 %0, %1, off sc1\n\ts_nop 1" ::"v"(p), "v"(v) : "memory"); }
DEV void st8_wt(void* p, f32x2 v) { asm volatile("global_store_dwordx2 %0, %1, off sc1" ::"v"(p), "v"(v) : "memory"); }
DEV int s0_of(int b) { return (b & 3) * 64 + ((b >> 2) & 1) * 32 + (b >> 3); }
#define CODE_ADDR(p, sym) do { const char* pc_; int off_; asm volatile("s_getpc_b64 %0\n\ts_mov_b32 %1, " #sym "@rel32@lo+4" : "=s"(pc_), "=s"(off_)); p = pc_ + off_; } while (0)
DEV void wave_fence() { asm volatile("" ::: "memory"); __builtin_amdgcn_wave_barrier(); }

DEV cf lds_ld(const char* base, int idx) { f32x2 v = *(const LAS f32x2*)((LAS const char*)base + idx * 8); return {v.x, v.y}; }
DEV void lds_st(char* base, int idx, cf a) { f32x2 v = {a.x, a.y}; *(LAS f32x2*)((LAS char*)base + idx * 8) = v; }

typedef f32x2 c2;
DEV c2 addi(c2 a, c2 b) { c2 r; asm("v_pk_add_f32 %0, %1, %2 op_sel:[0,1] op_sel_hi:[1,0] neg_lo:[0,1]" : "=v"(r) : "v"(a), "v"(b)); return r; }
DEV c2 subi(c2 a, c2 b) { c2 r; asm("v_pk_add_f32 %0, %1, %2 op_sel:[0,1] op_sel_hi:[1,0] neg_hi:[0,1]" : "=v"(r) : "v"(a), "v"(b)); return r; }
DEV c2 addc(c2 a, c2 b) { c2 r; asm("v_pk_add_f32 %0, %1, %2 neg_hi:[0,1]" : "=v"(r) : "v"(a), "v"(b)); return r; }
DEV c2 subc(c2 a, c2 b) { c2 r; asm("v_pk_add_f32 %0, %1, %2 neg_lo:[0,1]" : "=v"(r) : "v"(a), "v"(b)); return r; }
DEV c2 pmul(c2 a, c2 w) {
    c2 t = a.xx * w, r;
    asm("v_pk_fma_f32 %0, %1, %2, %3 op_sel:[1,1,0] op_sel_hi:[1,0,1] neg_lo:[0,1,0]" : "=v"(r) : "v"(a), "v"(w), "v"(t));
    return r;
}
DEV c2 pmulc(c2 a, c2 w) {
    c2 t = a.xx * w, r;
    asm("v_pk_fma_f32 %0, %1, %2, %3 op_sel:[1,1,0] op_sel_hi:[1,0,1] neg_hi:[0,1,0]" : "=v"(r) : "v"(a), "v"(w), "v"(t));
    return r;
}
DEV c2 pfma(c2 a, float k, c2 c) { return __builtin_elementwise_fma(a, c2{k, k}, c); }
DEV c2 ptw(float f) { c2 r = {__builtin_amdgcn_cosf(f), -__builtin_amdgcn_sinf(f)}; PIN(r); return r; }
DEV void pfft4(c2& a0, c2& a1, c2& a2, c2& a3) {
    c2 t0 = a0 + a2, t1 = a0 - a2, t2 = a1 + a3, t3 = a1 - a3;
    a0 = t0 + t2; a2 = t0 - t2; a1 = subi(t1, t3); a3 = addi(t1, t3);
}
DEV void pfft8(c2 (&a)[8]) {
    pfft4(a[0], a[2], a[4], a[6]);
    pfft4(a[1], a[3], a[5], a[7]);
    const c2 e0 = a[0], e1 = a[2], e2 = a[4], e3 = a[6], o0 = a[1], o2 = a[5];
    const c2 t1 = subi(a[3], a[3]), t3 = addi(a[7], a[7]);
    a[0] = e0 + o0; a[4] = e0 - o0;
    a[1] = pfma(t1, RH, e1); a[5] = pfma(t1, -RH, e1);
    a[2] = subi(e2, o2); a[6] = addi(e2, o2);
    a[3] = pfma(t3, -RH, e3); a[7] = pfma(t3, RH, e3);
}
DEV void pfft5(c2 (&a)[5]) {
    constexpr float c1 = 0.30901699437494742f, cc2 = -0.80901699437494742f;
    constexpr float s1 = 0.95105651629515357f, s2 = 0.58778525229247313f;
    const c2 t1 = a[1] + a[4], t2 = a[2] + a[3], t3 = a[1] - a[4], t4 = a[2] - a[3];
    const c2 m1 = pfma(t2, cc2, pfma(t1, c1, a[0])), m2 = pfma(t2, c1, pfma(t1, cc2, a[0]));
    const c2 n1 = pfma(t4, s2, t3 * s1), n2 = pfma(t4, -s1, t3 * s2);
    a[0] = a[0] + t1 + t2;
    a[1] = subi(m1, n1); a[4] = addi(m1, n1);
    a[2] = subi(m2, n2); a[3] = addi(m2, n2);
}
DEV void pfft10(c2 (&a)[10]) {
    c2 e[5], o[5];
#pragma unroll
    for (int n2 = 0; n2 < 5; ++n2) { const c2 p = a[(2 * n2) % 10], q = a[(2 * n2 + 5) % 10]; e[n2] = p + q; o[n2] = p - q; }
    pfft5(e); pfft5(o);
#pragma unroll
    for (int k2 = 0; k2 < 5; ++k2) { a[(6 * k2) % 10] = e[k2]; a[(6 * k2 + 5) % 10] = o[k2]; }
}
DEV c2 pld(const char* base, int idx) { return *(const LAS c2*)((LAS const char*)base + idx * 8); }
DEV void pst(char* base, int idx, c2 a) { *(LAS c2*)((LAS char*)base + idx * 8) = a; }
DEV unsigned pack_f16(c2 a) { return __builtin_bit_cast(unsigned, __builtin_convertvector(a, f16x2)); }

constexpr unsigned CANARY = 0x7f817f81u;
constexpr int K1_CODE = 7680, K2_CODE = 19200, K3_CODE = 10240;
constexpr int BUFA = 4 * 720 * 8, BUFB = 4 * 656 * 8, XP = 1296;

extern "C" __global__ __launch_bounds__(512, 2) void k1_kernel(const unsigned* __restrict__ x32, const char* __restrict__ w1, const char* __restrict__ w3,
                                                   const unsigned short* __restrict__ wr, const unsigned short* __restrict__ wi,
                                                   const char* __restrict__ w2, char* __restrict__ ws) {
    __shared__ __attribute__((aligned(16))) char smem[BUFA + BUFB];
    const int b = blockIdx.x, tid = threadIdx.x, lane = tid & 63;
    const int wv = __builtin_amdgcn_readfirstlane(tid >> 6);
    STAMP_DECL;
    STAMP(0, 0);
    if (__builtin_expect(b < 256, 1)) {
        const int s0 = s0_of(b);
        const int s = s0 + 256 * wv;
        unsigned z[5];
#pragma unroll
        for (int i = 0; i < 5; ++i) z[i] = x32[s * 320 + lane + 64 * i];
        unsigned cpf = 0u;
        if (wv == 7) {
            const char* pc; CODE_ADDR(pc, k1_kernel);
            cpf = *(const GAS unsigned*)(pc + (lane < K1_CODE / 128 ? lane : 0) * 128);
        }
        const int qr = tid / 80, r = tid - qr * 80;
        c2 t640[8], t80[8];
        if (wv < 5) {
#pragma unroll
            for (int a = 1; a < 8; ++a) t640[a] = ptw((float)(r * a) * (1.0f / 640.0f));
#pragma unroll
            for (int a = 1; a < 8; ++a) t80[a] = ptw((float)((r >> 3) * a) * (1.0f / 80.0f));
        }
        const c2 twq = ptw((float)(s0 * (lane & 7)) * (1.0f / 2048.0f));
        SB();
        float v[10];
#pragma unroll
        for (int i = 0; i < 5; ++i) { v[2 * i] = bf_lo(z[i]); v[2 * i + 1] = bf_hi(z[i]); }
        const float c0 = rdlane(v[0], 0);
        STAMP(0, 1);
        float s1 = 0.f, s2 = 0.f;
#pragma unroll
        for (int i = 0; i < 10; ++i) { float d = v[i] - c0; s1 += d; s2 += d * d; }
        s1 = wave_sum(s1); s2 = wave_sum(s2);
        const float md = s1 * (1.0f / 640.0f);
        float var = s2 * (1.0f / 640.0f) - md * md;
        var = var < 0.f ? 0.f : var;
        const float m = rbf(c0 + md);
        const float sd = rbf(__builtin_amdgcn_sqrtf(rbf(rbf(var) + 1e-5f)));
        const float rs = __builtin_amdgcn_rcpf(sd);
        char* bufA = smem;
        char* bufB = smem + BUFA;
#pragma unroll
        for (int i = 0; i < 5; ++i) {
            unsigned d = pack_bf16(v[2 * i] - m, v[2 * i + 1] - m);
            *(LAS unsigned*)((LAS char*)bufB + wv * XP + (lane + 64 * i) * 4) = pack_bf16(bf_lo(d) * rs, bf_hi(d) * rs);
        }
        wait_lgkm0();
        bar();
        STAMP(0, 2);
        if (wv >= 5) {
            u32x4 pc[4];
#pragma unroll
            for (int i = 0; i < 4; ++i) { const int p = tid - 320 + 192 * i, pp = p < 640 ? p : 0; pc[i] = *(const LAS u32x4*)((LAS const char*)bufB + (pp & 7) * XP + (pp >> 3) * 16); }
            wait_lgkm0();
            bar();
            char* xt = ws + WS_XN + (size_t)(s0 >> 4) * (20 * 8192) + ((s0 >> 2) & 3) * 1024 + (s0 & 3) * 128;
#pragma unroll
            for (int i = 0; i < 4; ++i) {
                const int p = tid - 320 + 192 * i, c = p >> 3;
                if (p < 640) *(u32x4*)(xt + (c >> 1) * 4096 + (c & 1) * 512 + (p & 7) * 16) = pc[i];
            }
            unsigned t1[3], t3[3], t2[3];
#pragma unroll
            for (int i = 0; i < 3; ++i) {
                const int L = (tid - 320) + 192 * i;
                const size_t off = (size_t)b * 7680 + (L < 480 ? L : 0) * 16;
                t1[i] = *(const unsigned*)(w1 + off); t3[i] = *(const unsigned*)(w3 + off);
                t2[i] = *(const unsigned*)(w2 + off);
            }
            unsigned cp2[2] = {0u, 0u};
            if (b < 32) {
                const char *p2, *p3; CODE_ADDR(p2, k2_kernel); CODE_ADDR(p3, k3_kernel);
                const int L = tid - 320;
                cp2[0] = *(const GAS unsigned*)(p2 + (L < K2_CODE / 128 ? L : 0) * 128);
                cp2[1] = *(const GAS unsigned*)(p3 + (L < K3_CODE / 128 ? L : 0) * 128);
            }
            bar(); bar();
            asm volatile("" :: "v"(cp2[0]), "v"(cp2[1]), "v"(cpf));
            asm volatile("" :: "v"(t2[0]), "v"(t2[1]), "v"(t2[2]), "v"(t1[0]), "v"(t1[1]), "v"(t1[2]), "v"(t3[0]), "v"(t3[1]), "v"(t3[2]));
            STAMP(0, 5); STAMP_FLUSH(0);
            return;
        }
        {
            c2 xr[8];
#pragma unroll
            for (int j = 0; j < 8; ++j) { const unsigned w = *(const LAS unsigned*)((LAS const char*)bufB + j * XP + tid * 4); xr[j] = c2{bf_lo(w), bf_hi(w)}; }
            const c2 a0 = xr[0] + xr[4], a1 = xr[0] - xr[4], b0 = xr[2] + xr[6], b1 = xr[2] - xr[6];
            const c2 e0 = xr[1] + xr[5], e1 = xr[1] - xr[5], d0 = xr[3] + xr[7], d1 = xr[3] - xr[7];
            const c2 p = a0 + b0, q = e0 + d0, u = e1 - d1, w = e1 + d1;
            const c2 y0 = p + q, y4 = p - q, y2r = a0 - b0, y2i = d0 - e0;
            const c2 y1r = pfma(u, RH, a1), y1i = pfma(w, -RH, -b1), y3r = pfma(u, -RH, a1), y3i = pfma(w, -RH, b1);
            *(LAS f32x4*)((LAS char*)bufA + (0 * 720 + 2 * tid) * 8) = f32x4{y0.x, y4.x, y0.y, y4.y};
            *(LAS f32x4*)((LAS char*)bufA + (1 * 720 + 2 * tid) * 8) = f32x4{y1r.x, y1i.x, y1r.y, y1i.y};
            *(LAS f32x4*)((LAS char*)bufA + (2 * 720 + 2 * tid) * 8) = f32x4{y2r.x, y2i.x, y2r.y, y2i.y};
            *(LAS f32x4*)((LAS char*)bufA + (3 * 720 + 2 * tid) * 8) = f32x4{y3r.x, y3i.x, y3r.y, y3i.y};
        }
        wait_lgkm0();
        bar();
        c2 a8[8];
        {
#pragma unroll
            for (int i = 0; i < 8; ++i) a8[i] = pld(bufA, qr * 720 + r + 80 * i);
            pfft8(a8);
            pst(bufB, qr * 656 + r, a8[0]);
#pragma unroll
            for (int a = 1; a < 8; ++a) pst(bufB, qr * 656 + a * 82 + r, pmul(a8[a], t640[a]));
        }
        wait_lgkm0();
        bar();
        STAMP(0, 3);
        {
            const int ib = qr * 656 + (r & 7) * 82 + (r >> 3), ob = qr * 720 + (r >> 3) * 72 + (r & 7);
#pragma unroll
            for (int i = 0; i < 8; ++i) a8[i] = pld(bufB, ib + 10 * i);
            pfft8(a8);
            pst(bufA, ob, a8[0]);
#pragma unroll
            for (int a = 1; a < 8; ++a) pst(bufA, ob + 8 * a, pmul(a8[a], t80[a]));
        }
        wait_lgkm0();
        bar();
        STAMP(0, 4);
        if (wv < 4) {
            c2 f[10];
#pragma unroll
            for (int t = 0; t < 10; ++t) f[t] = pld(bufA, wv * 720 + t * 72 + lane);
            pfft10(f);
            unsigned* T = (unsigned*)(ws + WS_T16) + (size_t)s0 * (8 * TP);
            if (wv == 0) {
#pragma unroll
                for (int bb = 5; bb < 10; ++bb) pst(bufB, lane + 64 * (bb - 5), f[bb]);
                if (lane == 0) pst(bufB, 320, f[0]);
                wave_fence();
                const c2 w4 = {rdlane(twq.y, 4) * 0.5f, rdlane(twq.x, 4) * -0.5f};
#pragma unroll
                for (int bb = 0; bb < 5; ++bb) {
                    const c2 h = pld(bufB, 320 - lane - 64 * bb);
                    T[lane + 64 * bb] = pack_f16(addc(f[bb], h) * 0.5f);
                    T[4 * TP + lane + 64 * bb] = pack_f16(pmul(subc(f[bb], h), w4));
                }
                if (lane == 0) {
                    T[320] = pack_f16(c2{f[5].x, 0.f});
                    T[4 * TP + 320] = pack_f16(pmul(subc(f[5], f[5]), w4));
                }
            } else {
                const c2 wa = {rdlane(twq.x, wv), rdlane(twq.y, wv)}, wb = {rdlane(twq.x, 8 - wv), rdlane(twq.y, 8 - wv)};
                unsigned* Ta = T + wv * TP;
                unsigned* Tb = T + (8 - wv) * TP;
#pragma unroll
                for (int bb = 0; bb < 5; ++bb) Ta[lane + 64 * bb] = pack_f16(pmul(f[bb], wa));
#pragma unroll
                for (int bb = 5; bb < 10; ++bb) Tb[640 - lane - 64 * bb] = pack_f16(pmulc(f[bb], wb));
                if (lane == 0) {
                    Tb[0] = pack_f16(pmulc(f[0], wb));
                    Ta[320] = pack_f16(pmul(f[5], wa));
                }
            }
        }
        STAMP(0, 5); STAMP_FLUSH(0);
    } else {
        const int id = b - 256, q = id & 7, a = id >> 3;
        const int k = tid;
        if (wv < 6) {
            unsigned o[16], mx = 0u;
            const int kc = k < DF_ ? k : DF_ - 1;
#pragma unroll
            for (int i = 0; i < 16; ++i) {
                const int u = 8 * (a + 16 * i) + q;
                const unsigned re = wr[u * DF_ + kc], im = wi[u * DF_ + kc];
                o[i] = re | (im << 16);
                const unsigned ma = re & 0x7fffu, mb = im & 0x7fffu;
                mx = mx > ma ? mx : ma; mx = mx > mb ? mx : mb;
            }
            mx = wave_max(mx);
            if (lane == 0) ((unsigned*)(ws + WS_WMAX))[id * 6 + wv] = mx;
          if (k < 4 * NG4) {
            u32x4* Wt = (u32x4*)(ws + WS_WT) + (q * NG4 + (k >> 2)) * 256 + a * 4 + (k & 3);
#pragma unroll
            for (int bq = 0; bq < 4; ++bq) Wt[bq * 64] = u32x4{o[4 * bq], o[4 * bq + 1], o[4 * bq + 2], o[4 * bq + 3]};
          }
        }
        STAMP(0, 5); STAMP_FLUSH(0);
    }
}

constexpr int WGRP = 3104, SLOT = 4 * WGRP, NS = 10, KSTEPS = 20;
constexpr int FSCR = 16 * 68 * 8;
#define FFT_GO 4u
constexpr int L2_F = NS * SLOT, L2_FLAG = L2_F + 3 * FSCR;
constexpr int L2_W2 = L2_FLAG + 64;
constexpr int K2_LDS = L2_W2 + 10240;

template <int N> DEV void wait_vm() { asm volatile("s_waitcnt vmcnt(%0)" ::"i"(N) : "memory"); }
DEV unsigned lds_min4(const char* smem, int off) {
    u32x4 c = *(const volatile LAS u32x4*)((LAS const char*)smem + off);
    unsigned a = c.x < c.y ? c.x : c.y, b = c.z < c.w ? c.z : c.w;
    return __builtin_amdgcn_readfirstlane(a < b ? a : b);
}
DEV unsigned lds_word(const char* smem, int off) { return __builtin_amdgcn_readfirstlane(*(const volatile LAS unsigned*)((LAS const char*)smem + off)); }
struct Dma { unsigned wo[3]; const char* w1b; const char* w3b; unsigned lw; char* smem; char* ws; };
constexpr int issued_before(int p) { int n = p == 0 ? 2 : p + 3; return n > KSTEPS ? KSTEPS : n; }
template <int ST> DEV void dma_stage(const Dma& d) {
    if constexpr (ST >= NS) {
        while (lds_min4(d.smem, L2_FLAG + 16) < (unsigned)(ST - NS + 1)) __builtin_amdgcn_s_sleep(1);
    }
#pragma unroll
    for (int ks = 0; ks < 2; ++ks) {
        const char* g1 = d.w1b + (size_t)(2 * ST + ks) * (16 * F_ * 2);
        const char* g3 = d.w3b + (size_t)(2 * ST + ks) * (16 * F_ * 2);
        const unsigned l0 = d.lw + (ST % NS) * SLOT + ks * (2 * WGRP), l1 = l0 + WGRP;
        asm volatile("s_mov_b32 m0, %5\n\ts_nop 0\n\t"
                     "global_load_lds_dwordx4 %0, %3\n\t"
                     "global_load_lds_dwordx4 %1, %3 offset:1024\n\t"
                     "global_load_lds_dwordx4 %2, %3 offset:2048\n\t"
                     "s_mov_b32 m0, %6\n\ts_nop 0\n\t"
                     "global_load_lds_dwordx4 %0, %4\n\t"
                     "global_load_lds_dwordx4 %1, %4 offset:1024\n\t"
                     "global_load_lds_dwordx4 %2, %4 offset:2048"
                     :: "v"(d.wo[0]), "v"(d.wo[1]), "v"(d.wo[2]), "s"(g1), "s"(g3), "s"(l0), "s"(l1) : "memory");
    }
}
template <int A, int B> DEV void dma_range(const Dma& d) { if constexpr (A < B) { dma_stage<A>(d); dma_range<A + 1, B>(d); } }
template <int P> DEV void dma_run(const Dma& d) {
    if constexpr (P < KSTEPS) {
        if constexpr (P > 0) dma_range<issued_before(P - 1), issued_before(P)>(d);
        wait_vm<12 * (issued_before(P) - 1 - P)>();
        *(volatile LAS unsigned*)((LAS char*)d.smem + L2_FLAG) = (unsigned)(P + 1);
        dma_run<P + 1>(d);
    }
}

DEV float silu_f(float v) { return v * __builtin_amdgcn_rcpf(1.0f + __builtin_amdgcn_exp2f(v * -1.44269504088896341f)); }
DEV u32x4 wfrag(const char* smem, unsigned a) {
    s16x4 lo = __builtin_amdgcn_ds_read_tr16_b64_v4i16((LAS s16x4*)((LAS char*)smem + a));
    s16x4 hi = __builtin_amdgcn_ds_read_tr16_b64_v4i16((LAS s16x4*)((LAS char*)smem + a + 768));
    return __builtin_bit_cast(u32x4, __builtin_shufflevector(lo, hi, 0, 1, 2, 3, 4, 5, 6, 7));
}

extern "C" __global__ NOPK __launch_bounds__(512) void k2_kernel(const char* __restrict__ w1, const char* __restrict__ w3, const char* __restrict__ w2,
                                                       char* __restrict__ ws) {
    extern __shared__ __attribute__((aligned(16))) char smem[];
    const int b = blockIdx.x, tid = threadIdx.x, lane = tid & 63;
    const int wv = __builtin_amdgcn_readfirstlane(tid >> 6);
    const int xcd = b & 7, idx = b >> 3;
    const int mt = (xcd & 3) * 4 + (idx & 3), nt = (xcd >> 2) * 8 + (idx >> 2);
    STAMP_DECL;
    STAMP(1, 0);
    if (wv == 0) {
        __builtin_amdgcn_s_setprio(2);
        Dma d;
#pragma unroll
        for (int r = 0; r < 3; ++r) {
            const int L = r * 64 + lane, row = L / 12, c = L - row * 12;
            d.wo[r] = (unsigned)(row * (F_ * 2) + c * 16 - r * 1024);
        }
        d.smem = smem; d.ws = ws;
        d.w1b = w1 + nt * 192; d.w3b = w3 + nt * 192;
        d.lw = (unsigned)(uintptr_t)(LAS char*)smem;
        if (tid < 16) *(volatile LAS unsigned*)((LAS char*)smem + L2_FLAG + tid * 4) = 0u;
        wait_lgkm0();
        dma_range<0, 1>(d);
        bar();
        dma_range<1, issued_before(0)>(d);
        STAMP(1, 1);
        dma_run<0>(d);
        STAMP(1, 11);
        unsigned wo[5];
#pragma unroll
        for (int r = 0; r < 5; ++r) {
            const int L = r * 64 + lane, row = L / 10, c = L - row * 10;
            wo[r] = (unsigned)(row * (D_ * 2) + c * 16 - (r & 3) * 1024);
        }
        const int nu = b < 128 ? 2 : 1;
        for (int i = 0; i < nu; ++i) {
            const int u = b + 256 * i, nt2 = u / 48, kb = u - nt2 * 48;
            const char* g = w2 + (size_t)kb * (32 * D_ * 2) + nt2 * 160;
            const unsigned l0 = d.lw + L2_W2 + i * 5120, l1 = l0 + 4096;
            asm volatile("s_mov_b32 m0, %6\n\ts_nop 0\n\t"
                         "global_load_lds_dwordx4 %0, %5\n\t"
                         "global_load_lds_dwordx4 %1, %5 offset:1024\n\t"
                         "global_load_lds_dwordx4 %2, %5 offset:2048\n\t"
                         "global_load_lds_dwordx4 %3, %5 offset:3072\n\t"
                         "s_mov_b32 m0, %7\n\ts_nop 0\n\t"
                         "global_load_lds_dwordx4 %4, %5"
                         :: "v"(wo[0]), "v"(wo[1]), "v"(wo[2]), "v"(wo[3]), "v"(wo[4]), "s"(g), "s"(l0), "s"(l1) : "memory");
        }
        wait_vm<0>();
        const int gq = lane >> 4;
        const unsigned ra = L2_W2 + (16 * (gq >> 1) + 4 * (gq & 1) + ((lane & 15) >> 2)) * 160 + (lane & 3) * 8;
        for (int i = 0; i < nu; ++i) {
            char* dst = ws + WS_W2T + (size_t)(b + 256 * i) * 5120 + lane * 16;
#pragma unroll
            for (int j = 0; j < 5; ++j) {
                s16x4 lo = __builtin_amdgcn_ds_read_tr16_b64_v4i16((LAS s16x4*)((LAS char*)smem + ra + i * 5120 + j * 32));
                s16x4 hi = __builtin_amdgcn_ds_read_tr16_b64_v4i16((LAS s16x4*)((LAS char*)smem + ra + i * 5120 + j * 32 + 8 * 160));
                st16_wt(dst + j * 1024, __builtin_bit_cast(u32x4, __builtin_shufflevector(lo, hi, 0, 1, 2, 3, 4, 5, 6, 7)));
            }
        }
        STAMP(1, 12); STAMP_FLUSH(1);
    } else if (wv >= 4) {
        __builtin_amdgcn_s_setprio(3);
        const int mw = wv - 4;
        const int h = lane >> 5, r32 = lane & 31;
        const int l16 = lane * 16;
        const __amdgpu_buffer_rsrc_t xr = __builtin_amdgcn_make_buffer_rsrc((void*)(ws + WS_XN + (size_t)mt * (20 * 8192) + mw * 1024), 0, 20 * 8192, 0x00020000);
        u32x4 xf[5][2];
#pragma unroll
        for (int st = 0; st < 5; ++st) {
            xf[st][0] = __builtin_amdgcn_raw_buffer_load_b128(xr, l16, st * 8192, 0);
            xf[st][1] = __builtin_amdgcn_raw_buffer_load_b128(xr, l16, st * 8192 + 4096, 0);
            if (st == 0) bar();
        }
        f32x16 acc[6];
#pragma unroll
        for (int i = 0; i < 6; ++i)
#pragma unroll
            for (int r = 0; r < 16; ++r) acc[i][r] = 0.f;
        u32x4 wa[6], wb[6];
        unsigned rc = ((lane >> 4) & 1) * WGRP + (8 * h + ((lane & 15) >> 2)) * 192 + (lane & 3) * 8, rn = rc + 5 * SLOT;
        unsigned fa = L2_FLAG, da = L2_FLAG + 16 + mw * 4, dv = 0;
        PIN(rc); PIN(rn); PIN(fa); PIN(da); PIN(dv);
#define MM(i, w, x) asm volatile("v_mfma_f32_32x32x16_bf16 %0, %1, %2, %0" : "+v"(acc[i]) : "v"(w), "v"(x) : "memory")
        while (lds_word(smem, L2_FLAG) < 1u) __builtin_amdgcn_s_sleep(1);
        STAMP(1, 1);
#pragma unroll
        for (int i = 0; i < 6; ++i) wa[i] = wfrag(smem, rc + i * 32);
#pragma unroll 1
        for (int io = 0; io < 3; ++io) {
#pragma unroll
            for (int ii = 0; ii < 5; ++ii) {
                const int it = io * 5 + ii;
                const unsigned cb = rc + ii * SLOT + 2 * WGRP, nb = ii < 4 ? rc + (ii + 1) * SLOT : rn;
                MM(0, wa[0], xf[ii][0]);
                wb[0] = wfrag(smem, cb);
                unsigned fl = *(const volatile LAS unsigned*)((LAS const char*)smem + fa);
                MM(1, wa[1], xf[ii][0]);
                wb[1] = wfrag(smem, cb + 32);
                MM(2, wa[2], xf[ii][0]);
                wb[2] = wfrag(smem, cb + 64);
                MM(3, wa[3], xf[ii][0]);
                wb[3] = wfrag(smem, cb + 96);
                asm volatile("v_add_u32 %0, 1, %0" : "+v"(dv));
                MM(4, wa[4], xf[ii][0]);
                wb[4] = wfrag(smem, cb + 128);
                {
                    unsigned m = __builtin_amdgcn_readfirstlane(fl);
                    const unsigned need = it == 14 ? KSTEPS : it + 2;
                    while (m < need) { __builtin_amdgcn_s_sleep(1); m = lds_word(smem, L2_FLAG); }
                }
                MM(5, wa[5], xf[ii][0]);
                wb[5] = wfrag(smem, cb + 160);
                MM(0, wb[0], xf[ii][1]);
                wa[0] = wfrag(smem, nb);
                xf[ii][0] = __builtin_amdgcn_raw_buffer_load_b128(xr, l16, (it + 5) * 8192, 0);
                MM(1, wb[1], xf[ii][1]);
                wa[1] = wfrag(smem, nb + 32);
                MM(2, wb[2], xf[ii][1]);
                wa[2] = wfrag(smem, nb + 64);
                MM(3, wb[3], xf[ii][1]);
                wa[3] = wfrag(smem, nb + 96);
                MM(4, wb[4], xf[ii][1]);
                wa[4] = wfrag(smem, nb + 128);
                MM(5, wb[5], xf[ii][1]);
                wa[5] = wfrag(smem, nb + 160);
                if ((it & 1) && io < 2) *(volatile LAS unsigned*)((LAS char*)smem + da) = dv;
                xf[ii][1] = __builtin_amdgcn_raw_buffer_load_b128(xr, l16, (it + 5) * 8192 + 4096, 0);
            }
            { const unsigned t = rc; rc = rn; rn = t; }
            STAMP(1, 2 + io); CSTAMP(1, 2 + io);
        }
        char* ht = ws + WS_HT + (size_t)((((mt * 2 + (mw >> 1)) * 48 + 3 * nt) * 4 + 2 * (mw & 1) + (r32 >> 4)) * 64 + (r32 & 15) + 16 * h) * 16;
        u32x4 g0[9], g1[9];
#define TFRAG(i, n) wfrag(smem, rc + ((n) >> 1) * SLOT + ((n) & 1) * (2 * WGRP) + (i) * 32)
#pragma unroll
        for (int n = 1; n < 10; ++n) g0[n - 1] = TFRAG(0, n);
#pragma unroll
        for (int i = 0; i < 7; ++i) {
            float hh[8];
#pragma unroll
            for (int n = 0; n < 10; ++n) {
                if (i < 6) {
                    if (n == 0) MM(i, wa[i], xf[0][0]);
                    else if (i & 1) MM(i, g1[n - 1], xf[n >> 1][n & 1]);
                    else MM(i, g0[n - 1], xf[n >> 1][n & 1]);
                    if (i < 5 && n < 9) { if (i & 1) g0[n] = TFRAG(i + 1, n + 1); else g1[n] = TFRAG(i + 1, n + 1); }
                } else if (n == 0) {
                    asm volatile("s_nop 15\n\ts_nop 7" ::: "memory");
                }
                if (i > 0) {
                    if (n >= 1 && n <= 8) { hh[n - 1] = silu_f(acc[i - 1][n - 1]) * acc[i - 1][8 + n - 1]; PIN(hh[n - 1]); }
                    if (n == 9) {
                        const u32x4 o = {pack_bf16(hh[0], hh[1]), pack_bf16(hh[2], hh[3]), pack_bf16(hh[4], hh[5]), pack_bf16(hh[6], hh[7])};
                        st16_wt(ht + ((i - 1) >> 1) * 4096 + ((i - 1) & 1) * 512, o);
                    }
                }
                SB();
            }
        }
        STAMP(1, 12); STAMP_FLUSH(1);
    } else {
        const int fw = wv - 1;
        const int q = xcd, g4 = fw * 32 + idx;
        if (g4 >= NG4) return;
        bar();
        const int t = lane >> 2;
        cf tw[16];
#pragma unroll
        for (int a = 1; a < 16; ++a) { tw[a] = twid((float)(t * a) * (1.0f / 256.0f)); PIN(tw[a].x); PIN(tw[a].y); }
        SB();
        while (lds_word(smem, L2_FLAG) < FFT_GO) __builtin_amdgcn_s_sleep(2);
        const int eo = (t * 8 + q) * TP + 4 * g4 + (lane & 3);
        const __amdgpu_buffer_rsrc_t tr = __builtin_amdgcn_make_buffer_rsrc((void*)(ws + WS_T16), 0, 2048 * TP * 4, 0x00020000);
        const __amdgpu_buffer_rsrc_t vr = __builtin_amdgcn_make_buffer_rsrc((void*)(ws + WS_T), 0, 2048 * TP * 4, 0x00020000);
        unsigned tv[16];
        u32x4 wq[4];
#pragma unroll
        for (int i = 0; i < 16; ++i) tv[i] = __builtin_amdgcn_raw_buffer_load_b32(tr, eo * 4, i * (128 * TP * 4), 0);
#pragma unroll
        for (int i = 0; i < 4; ++i) wq[i] = ((const u32x4*)(ws + WS_WT))[(size_t)(q * NG4 + g4) * 256 + i * 64 + lane];
        const WMax wm = vscale_load(ws, lane);
        SB();
        char* fs = smem + L2_F + fw * FSCR;
        const float vs = __builtin_bit_cast(float, (unsigned)(254 - vscale_exp(wm)) << 23);
        cf v[16];
#pragma unroll
        for (int i = 0; i < 16; ++i) { const f16x2 hv = __builtin_bit_cast(f16x2, tv[i]); v[i] = {(float)hv.x, (float)hv.y}; }
        STAMP(1, 1);
        fft16_a<-1>(v); fft16_t<-1>(v); fft16_b<-1>(v);
        lds_st(fs, lane, v[0]);
#pragma unroll
        for (int a = 1; a < 16; ++a) lds_st(fs, a * 68 + lane, cmul(v[a], tw[a]));
        wave_fence();
#pragma unroll
        for (int i = 0; i < 16; ++i) v[i] = lds_ld(fs, t * 68 + i * 4 + (lane & 3));
        fft16_a<-1>(v); fft16_t<-1>(v); fft16_b<-1>(v);
#pragma unroll
        for (int i = 0; i < 16; ++i) { const unsigned w = wq[i >> 2][i & 3]; v[i] = cmul(v[i], cf{bf_lo(w), bf_hi(w)}); }
        if (fw < 2) {
            char* sp = ws + WS_S + (size_t)b * 10240 + fw * 5120 + lane * 16;
#pragma unroll
            for (int i = 0; i < 5; ++i) st16_wt(sp + i * 1024, u32x4{CANARY, CANARY, CANARY, CANARY});
        }
        fft16_a<1>(v); fft16_t<1>(v); fft16_b<1>(v);
        wave_fence();
        lds_st(fs, t * 68 + (lane & 3), v[0]);
#pragma unroll
        for (int i = 1; i < 16; ++i) lds_st(fs, t * 68 + i * 4 + (lane & 3), cmulc(v[i], tw[i]));
        wave_fence();
#pragma unroll
        for (int a = 0; a < 16; ++a) v[a] = lds_ld(fs, a * 68 + lane);
        fft16_a<1>(v); fft16_t<1>(v); fft16_b<1>(v);
#pragma unroll
        for (int i = 0; i < 16; ++i)
            __builtin_amdgcn_raw_buffer_store_b32(__builtin_bit_cast(unsigned, f16x2{(_Float16)(v[i].x * vs), (_Float16)(v[i].y * vs)}), vr, eo * 4, i * (128 * TP * 4), 0);
        STAMP(1, 12); STAMP_FLUSH(1);
    }
}

constexpr int RPITCH = 336;
constexpr int L3_RED = 0;
constexpr int L3_A = 4 * 64 * RPITCH;
constexpr int L3_B = L3_A + 4 * 720 * 8;
constexpr int L3_CNT = L3_B + 4 * 656 * 8;
constexpr int K3_LDS = L3_CNT + 64;
#define GO_STEP 9

DEV void helper_barrier(char* smem, int lane, unsigned target) {
    wait_lgkm0();
    LAS unsigned* cnt = (LAS unsigned*)((LAS char*)smem + L3_CNT);
    if (lane == 0) __hip_atomic_fetch_add(cnt, 1u, __ATOMIC_RELAXED, __HIP_MEMORY_SCOPE_WORKGROUP);
    while (__hip_atomic_load(cnt, __ATOMIC_RELAXED, __HIP_MEMORY_SCOPE_WORKGROUP) < target) __builtin_amdgcn_s_sleep(1);
    asm volatile("" ::: "memory");
}
#define MFMA16(a, b, c) __builtin_amdgcn_mfma_f32_16x16x32_bf16(__builtin_bit_cast(bf16x8, a), __builtin_bit_cast(bf16x8, b), c, 0, 0, 0)

extern "C" __global__ NOPK __launch_bounds__(768) void k3_kernel(const char* __restrict__ x, char* __restrict__ ws, char* __restrict__ out) {
    extern __shared__ __attribute__((aligned(16))) char smem[];
    const int b = blockIdx.x, tid = threadIdx.x, lane = tid & 63;
    const int wv = __builtin_amdgcn_readfirstlane(tid >> 6);
    const int xcd = b & 7, idx = b >> 3;
    const int mt2 = (xcd & 3) * 8 + (idx & 7), nt2 = (xcd >> 2) * 4 + (idx >> 3);
    STAMP_DECL;
    STAMP(2, 0);
    if (tid < 8) *(LAS unsigned*)((LAS char*)smem + L3_CNT + tid * 4) = 0u;
    wait_lgkm0();
    bar();
    if (wv >= 8) {
        __builtin_amdgcn_s_setprio(2);
        const int gw = wv - 8;
        const char* hb = ws + WS_HT + (size_t)(mt2 * 48 + 12 * gw) * 4096;
        const char* wb = ws + WS_W2T + (size_t)(nt2 * 48 + 12 * gw) * 5120;
        const int l16 = lane * 16;
        const __amdgpu_buffer_rsrc_t hr = __builtin_amdgcn_make_buffer_rsrc((void*)hb, 0, 12 * 4096, 0x00020000);
        const __amdgpu_buffer_rsrc_t wr_ = __builtin_amdgcn_make_buffer_rsrc((void*)wb, 0, 12 * 5120, 0x00020000);
#define HLD(st, tb) __builtin_amdgcn_raw_buffer_load_b128(hr, l16 + (tb) * 1024, (st) * 4096, 0)
#define WLD(st, j) __builtin_amdgcn_raw_buffer_load_b128(wr_, l16 + ((j) & 3) * 1024, (st) * 5120 + ((j) >> 2) * 4096, 0)
        CSTAMP(2, 0);
        u32x4 hf[2][4], wf[2][5];
#pragma unroll
        for (int st = 0; st < 2; ++st) {
#pragma unroll
            for (int tb = 0; tb < 4; ++tb) hf[st][tb] = HLD(st, tb);
#pragma unroll
            for (int j = 0; j < 5; ++j) wf[st][j] = WLD(st, j);
            SB();
        }
        f32x4 acc[5][4];
#pragma unroll
        for (int j = 0; j < 5; ++j)
#pragma unroll
            for (int tb = 0; tb < 4; ++tb) acc[j][tb] = f32x4{0.f, 0.f, 0.f, 0.f};
        char* rp = smem + L3_RED + gw * (64 * RPITCH) + (lane & 15) * RPITCH + (lane >> 4) * 16;
        SB();
#pragma unroll
        for (int st = 0; st < 12; ++st) {
            const int sl = st & 1;
            const bool more = st + 2 < 12;
#pragma unroll
            for (int j = 0; j < 4; ++j) {
#pragma unroll
                for (int tb = 0; tb < 4; ++tb) acc[j][tb] = MFMA16(wf[sl][j], hf[sl][tb], acc[j][tb]);
                SB();
                if (more) wf[sl][j] = WLD(st + 2, j);
                if (st == 11 && j > 0) {
#pragma unroll
                    for (int tb = 0; tb < 4; ++tb) *(LAS f32x4*)((LAS char*)rp + tb * (16 * RPITCH) + (j - 1) * 64) = acc[j - 1][tb];
                }
                SB();
            }
#pragma unroll
            for (int tb = 0; tb < 4; ++tb) {
                acc[4][tb] = MFMA16(wf[sl][4], hf[sl][tb], acc[4][tb]);
                SB();
                if (more) hf[sl][tb] = HLD(st + 2, tb);
                SB();
            }
            if (more) wf[sl][4] = WLD(st + 2, 4);
            if (st == GO_STEP) *(volatile LAS unsigned*)((LAS char*)smem + L3_CNT + 16 + gw * 4) = 1u;
            SB();
        }
        STAMP(2, 1); CSTAMP(2, 1);
#pragma unroll
        for (int j = 3; j < 5; ++j)
#pragma unroll
            for (int tb = 0; tb < 4; ++tb) *(LAS f32x4*)((LAS char*)rp + tb * (16 * RPITCH) + j * 64) = acc[j][tb];
        wait_lgkm0();
        STAMP(2, 2);
        bar();
        STAMP_FLUSH(2);
    } else {
        __builtin_amdgcn_s_setprio(3);
        const int s0 = s0_of(b);
        const f16x2* V = (const f16x2*)(ws + WS_T) + (size_t)s0 * (8 * TP);
        char* sg = ws + WS_S;
        f16x2 u[10], u2[10], e0, e1;
        const WMax wm = vscale_load(ws, lane);
        if (wv < 4) {
            const int qa = wv, qb = wv == 0 ? 0 : 8 - wv;
#pragma unroll
            for (int bb = 0; bb < 5; ++bb) u[bb] = V[qa * TP + lane + 64 * bb];
#pragma unroll
            for (int bb = 5; bb < 10; ++bb) u[bb] = V[qb * TP + 640 - lane - 64 * bb];
            if (wv == 0) {
#pragma unroll
                for (int bb = 0; bb < 5; ++bb) u2[bb] = V[4 * TP + lane + 64 * bb];
#pragma unroll
                for (int bb = 5; bb < 10; ++bb) u2[bb] = V[4 * TP + 640 - lane - 64 * bb];
            } else {
                e0 = V[qb * TP];
                e1 = V[qa * TP + 320];
            }
        }
        SB();
        __builtin_amdgcn_s_setprio(0);
        STAMP(2, 12);
        if (wv < 5) {
            const int qr = tid / 80, r = tid - qr * 80;
            cf t80[10], t640[8];
#pragma unroll
            for (int t = 1; t < 10; ++t) { t80[t] = twid((float)((lane >> 3) * t) * (1.0f / 80.0f)); PIN(t80[t].x); PIN(t80[t].y); }
#pragma unroll
            for (int a = 1; a < 8; ++a) { t640[a] = twid((float)(r * a) * (1.0f / 640.0f)); PIN(t640[a].x); PIN(t640[a].y); }
            cf twq = twid((float)(s0 * (lane & 7)) * (1.0f / 2048.0f));
            SB();
            STAMP(2, 11);
            char* bufA = smem + L3_A;
            char* bufB = smem + L3_B;
            if (wv < 4) {
                const int ve = vscale_exp(wm);
                const float NORM = (1.0f / (2048.0f * 640.0f)) * __builtin_bit_cast(float, (unsigned)ve << 23);
                cf f[10];
                if (wv == 0) {
                    const cf w4 = {rdlane(twq.x, 4) * NORM, rdlane(twq.y, 4) * NORM};
#pragma unroll
                    for (int bb = 0; bb < 10; ++bb) {
                        cf A = {(float)u[bb].x * NORM, (float)u[bb].y * NORM}, B = cmulc(cf{(float)u2[bb].x, (float)u2[bb].y}, w4);
                        if (bb == 0 || bb == 5) { if (lane == 0) { A.y = 0.f; B.y = 0.f; } }
                        f[bb] = bb < 5 ? cf{A.x - B.y, A.y + B.x} : cf{A.x + B.y, B.x - A.y};
                    }
                } else {
                    const float N2 = 2.0f * NORM;
                    const cf wa = {rdlane(twq.x, wv) * N2, rdlane(twq.y, wv) * N2}, wb = {rdlane(twq.x, 8 - wv) * N2, rdlane(twq.y, 8 - wv) * N2};
#pragma unroll
                    for (int bb = 0; bb < 5; ++bb) f[bb] = cmulc(cf{(float)u[bb].x, (float)u[bb].y}, wa);
#pragma unroll
                    for (int bb = 5; bb < 10; ++bb) { cf t = cmulc(cf{(float)u[bb].x, (float)u[bb].y}, wb); f[bb] = cf{t.x, -t.y}; }
                    const cf g0 = cmulc(cf{(float)e0.x, (float)e0.y}, wb), g1 = cmulc(cf{(float)e1.x, (float)e1.y}, wa);
                    if (lane == 0) {
                        f[0] = cf{0.5f * (f[0].x + g0.x), 0.5f * (f[0].y - g0.y)};
                        f[5] = cf{0.5f * (f[5].x + g1.x), 0.5f * (f[5].y + g1.y)};
                    }
                }
                STAMP(2, 1);
                fft10<1>(f);
                lds_st(bufA, wv * 720 + lane, f[0]);
#pragma unroll
                for (int t = 1; t < 10; ++t) lds_st(bufA, wv * 720 + t * 72 + lane, cmulc(f[t], t80[t]));
            }
            STAMP(2, 2);
            helper_barrier(smem, lane, 5);
            STAMP(2, 13);
            cf a8[8];
            {
                const int base = qr * 720 + (r >> 3) * 72 + (r & 7);
#pragma unroll
                for (int i = 0; i < 8; ++i) a8[i] = lds_ld(bufA, base + 8 * i);
                fft8<1>(a8);
                const int ob = qr * 656 + (r & 7) * 82 + (r >> 3);
#pragma unroll
                for (int i = 0; i < 8; ++i) lds_st(bufB, ob + 10 * i, a8[i]);
            }
            STAMP(2, 14);
            helper_barrier(smem, lane, 10);
            STAMP(2, 15);
            {
                a8[0] = lds_ld(bufB, qr * 656 + r);
#pragma unroll
                for (int a = 1; a < 8; ++a) a8[a] = cmulc(lds_ld(bufB, qr * 656 + a * 82 + r), t640[a]);
                fft8<1>(a8);
#pragma unroll
                for (int i = 0; i < 8; ++i) lds_st(bufA, qr * 720 + r + 80 * i, a8[i]);
            }
            helper_barrier(smem, lane, 15);
            STAMP(2, 3);
            {
                f32x4 p[4];
#pragma unroll
                for (int q = 0; q < 4; ++q) p[q] = *(const LAS f32x4*)((LAS const char*)bufA + (q * 720 + 2 * tid) * 8);
                float y[2][8];
#pragma unroll
                for (int e = 0; e < 2; ++e) {
                    const float P0 = p[0][2 * e], P4 = p[0][2 * e + 1];
                    const float A = P0 + P4, B = P0 - P4;
                    const float c0 = A + p[2][2 * e], c2 = A - p[2][2 * e], c1 = B - p[2][2 * e + 1], c3 = B + p[2][2 * e + 1];
                    const float o0 = p[1][2 * e] + p[3][2 * e], o2 = p[3][2 * e + 1] - p[1][2 * e + 1];
                    const float uu = p[1][2 * e] - p[3][2 * e], vv = p[1][2 * e + 1] + p[3][2 * e + 1];
                    const float o1 = uu - vv, o3 = -uu - vv;
                    y[e][0] = c0 + o0; y[e][4] = c0 - o0;
                    y[e][2] = c2 + o2; y[e][6] = c2 - o2;
                    y[e][1] = c1 + RH * o1; y[e][5] = c1 - RH * o1;
                    y[e][3] = c3 + RH * o3; y[e][7] = c3 - RH * o3;
                }
                char* stg = bufB + wv * 2048;
#pragma unroll
                for (int j = 0; j < 8; ++j) *(LAS unsigned*)((LAS char*)stg + j * 256 + lane * 4) = pack_bf16(y[0][j], y[1][j]);
                wave_fence();
                __builtin_amdgcn_s_setprio(3);
#pragma unroll
                for (int hh = 0; hh < 2; ++hh) {
                    const int j = 4 * hh + (lane >> 4);
                    u32x4 o = *(const LAS u32x4*)((LAS const char*)stg + hh * 1024 + lane * 16);
                    st16_wt(sg + (size_t)(s0 + 256 * j) * (D_ * 2) + wv * 256 + (lane & 15) * 16, o);
                }
            }
            __builtin_amdgcn_s_setprio(0);
            STAMP(2, 4);
        }
        const int r0 = tid / 10, c0 = tid - r0 * 10, p1 = 512 + (tid >> 2), r1 = p1 / 10, c1 = p1 - r1 * 10;
        const size_t eo0 = (size_t)(mt2 * 8 + (r0 >> 3) + 256 * (r0 & 7)) * (D_ * 2) + nt2 * 160 + c0 * 16;
        const size_t eo1 = (size_t)(mt2 * 8 + (r1 >> 3) + 256 * (r1 & 7)) * (D_ * 2) + nt2 * 160 + c1 * 16 + (tid & 3) * 4;
        SB();
        while (lds_min4(smem, L3_CNT + 16) == 0u) __builtin_amdgcn_s_sleep(2);
        __builtin_amdgcn_s_setprio(3);
        STAMP(2, 7);
        const u32x4 xa = *(const u32x4*)(x + eo0);
        const unsigned xb = *(const unsigned*)(x + eo1);
        u32x4 sa; unsigned sb;
        {
            const char* src0 = sg + eo0;
            const char* src1 = sg + eo1;
            for (unsigned spin = 0; spin < (1u << 20); ++spin) {
                asm volatile("global_load_dwordx4 %0, %2, off sc1\n\tglobal_load_dword %1, %3, off sc1\n\ts_waitcnt vmcnt(0)"
                             : "=&v"(sa), "=&v"(sb) : "v"(src0), "v"(src1) : "memory");
                const bool bad = sa.x == CANARY || sa.y == CANARY || sa.z == CANARY || sa.w == CANARY || sb == CANARY;
                if (__builtin_amdgcn_ballot_w64(bad) == 0ull) break;
                __builtin_amdgcn_s_sleep(8);
            }
        }
        STAMP(2, 8);
        bar();
        STAMP(2, 9);
        {
            const char* rp = smem + L3_RED + r0 * RPITCH + c0 * 32;
            const char* rq = smem + L3_RED + r1 * RPITCH + c1 * 32 + (tid & 3) * 8;
            f32x4 o0 = *(const LAS f32x4*)((LAS const char*)rp), o1 = *(const LAS f32x4*)((LAS const char*)rp + 16);
            f32x2 o2 = *(const LAS f32x2*)((LAS const char*)rq);
#pragma unroll
            for (int w = 1; w < 4; ++w) {
                o0 += *(const LAS f32x4*)((LAS const char*)rp + w * (64 * RPITCH));
                o1 += *(const LAS f32x4*)((LAS const char*)rp + w * (64 * RPITCH) + 16);
                o2 += *(const LAS f32x2*)((LAS const char*)rq + w * (64 * RPITCH));
            }
            const float o[8] = {o0.x, o0.y, o0.z, o0.w, o1.x, o1.y, o1.z, o1.w};
            u32x4 res;
#pragma unroll
            for (int i = 0; i < 4; ++i)
                res[i] = pack_bf16(bf_lo(xa[i]) + bf_lo(sa[i]) * silu_f(o[2 * i]), bf_hi(xa[i]) + bf_hi(sa[i]) * silu_f(o[2 * i + 1]));
            *(u32x4*)(out + eo0) = res;
            *(unsigned*)(out + eo1) = pack_bf16(bf_lo(xb) + bf_lo(sb) * silu_f(o2.x), bf_hi(xb) + bf_hi(sb) * silu_f(o2.y));
        }
        STAMP(2, 10); STAMP_FLUSH(2);
    }
}

extern "C" void kernel_launch(void* const* d_in, const int* in_sizes, int n_in, void* d_out, int out_size, void* d_ws,
                              size_t ws_size, hipStream_t stream) {
    (void)in_sizes; (void)n_in; (void)out_size; (void)ws_size;
    char* ws = (char*)d_ws;
    (void)hipFuncSetAttribute((const void*)k2_kernel, hipFuncAttributeMaxDynamicSharedMemorySize, K2_LDS);
    (void)hipFuncSetAttribute((const void*)k3_kernel, hipFuncAttributeMaxDynamicSharedMemorySize, K3_LDS);
    k1_kernel<<<384, 512, 0, stream>>>((const unsigned*)d_in[0], (const char*)d_in[4], (const char*)d_in[5], (const unsigned short*)d_in[1],
                                       (const unsigned short*)d_in[2], (const char*)d_in[6], ws);
    k2_kernel<<<256, 512, K2_LDS, stream>>>((const char*)d_in[4], (const char*)d_in[5], (const char*)d_in[6], ws);
    k3_kernel<<<256, 768, K3_LDS, stream>>>((const char*)d_in[0], ws, (char*)d_out);
}
```

```cpp
#include <hip/hip_runtime.h>
#include <hip/hip_bf16.h>
#include <stdint.h>
#define TOUCH_W2

typedef float f32x2 __attribute__((ext_vector_type(2)));
typedef float f32x4 __attribute__((ext_vector_type(4)));
typedef float f32x16 __attribute__((ext_vector_type(16)));
typedef short s16x4 __attribute__((ext_vector_type(4)));
typedef short s16x8 __attribute__((ext_vector_type(8)));
typedef __bf16 bf16x2 __attribute__((ext_vector_type(2)));
typedef __bf16 bf16x8 __attribute__((ext_vector_type(8)));
typedef _Float16 f16x2 __attribute__((ext_vector_type(2)));
typedef unsigned u32x2 __attribute__((ext_vector_type(2)));
typedef unsigned u32x4 __attribute__((ext_vector_type(4)));

#define DEV __device__ __forceinline__
#define LAS __attribute__((address_space(3)))
#define GAS __attribute__((address_space(1)))
#if defined(__HIP_DEVICE_COMPILE__)
#define NOPK __attribute__((target("no-packed-fp32-ops")))
#else
#define NOPK
#endif

constexpr int S_ = 2048, D_ = 640, F_ = 1536, DF_ = 321;
constexpr int NG4 = 81;
constexpr int NUNIT = 8 * NG4;

constexpr size_t WS_XN = 0;
constexpr size_t WS_T = 4u << 20;
constexpr size_t WS_WT = 12u << 20;
constexpr size_t WS_W2T = 16u << 20;
constexpr size_t WS_HT = 20u << 20;
constexpr size_t WS_S = 28u << 20;
constexpr size_t WS_T16 = 32u << 20;
constexpr size_t WS_SYNC = 37u << 20;
constexpr size_t WS_WMAX = 36u << 20;
constexpr int TP = 328;

constexpr size_t WS_DBG = 64u << 20;
#define STAMP_DECL
#define STAMP(kern, id) do {} while (0)
#define CSTAMP(kern, id) do {} while (0)
#define STAMP_FLUSH(kern) do {} while (0)

struct cf { float x, y; };
DEV cf operator+(cf a, cf b) { return {a.x + b.x, a.y + b.y}; }
DEV cf operator-(cf a, cf b) { return {a.x - b.x, a.y - b.y}; }
DEV cf cmul(cf a, cf w) { return {a.x * w.x - a.y * w.y, a.x * w.y + a.y * w.x}; }
DEV cf cmulc(cf a, cf w) { return {a.x * w.x + a.y * w.y, a.y * w.x - a.x * w.y}; }
template <int DIR> DEV cf muli(cf a) { return DIR > 0 ? cf{-a.y, a.x} : cf{a.y, -a.x}; }
DEV cf twid(float f) { return {__builtin_amdgcn_cosf(f), -__builtin_amdgcn_sinf(f)}; }

DEV unsigned pack_bf16(float a, float b) {
    f32x2 v = {a, b};
    return __builtin_bit_cast(unsigned, __builtin_convertvector(v, bf16x2));
}
DEV float bf_lo(unsigned u) { return __uint_as_float(u << 16); }
DEV float bf_hi(unsigned u) { return __uint_as_float(u & 0xffff0000u); }
DEV float rbf(float a) { return bf_lo(pack_bf16(a, a)); }

template <int DIR> DEV void fft4(cf& a0, cf& a1, cf& a2, cf& a3) {
    cf t0 = a0 + a2, t1 = a0 - a2, t2 = a1 + a3, t3 = muli<DIR>(a1 - a3);
    a0 = t0 + t2; a2 = t0 - t2; a1 = t1 + t3; a3 = t1 - t3;
}
constexpr float RH = 0.70710678118654752f;
template <int DIR> DEV cf mulw8(cf a) { return DIR > 0 ? cf{(a.x - a.y) * RH, (a.x + a.y) * RH} : cf{(a.x + a.y) * RH, (a.y - a.x) * RH}; }
template <int DIR> DEV cf mulw83(cf a) { return DIR > 0 ? cf{-(a.x + a.y) * RH, (a.x - a.y) * RH} : cf{(a.y - a.x) * RH, -(a.x + a.y) * RH}; }

template <int DIR> DEV void fft8(cf (&a)[8]) {
    fft4<DIR>(a[0], a[2], a[4], a[6]);
    fft4<DIR>(a[1], a[3], a[5], a[7]);
    cf o1 = mulw8<DIR>(a[3]), o2 = muli<DIR>(a[5]), o3 = mulw83<DIR>(a[7]);
    cf e0 = a[0], e1 = a[2], e2 = a[4], e3 = a[6], o0 = a[1];
    a[0] = e0 + o0; a[4] = e0 - o0;
    a[1] = e1 + o1; a[5] = e1 - o1;
    a[2] = e2 + o2; a[6] = e2 - o2;
    a[3] = e3 + o3; a[7] = e3 - o3;
}
template <int DIR> DEV void fft5(cf (&a)[5]) {
    constexpr float c1 = 0.30901699437494742f, c2 = -0.80901699437494742f;
    constexpr float s1 = 0.95105651629515357f, s2 = 0.58778525229247313f;
    cf t1 = a[1] + a[4], t2 = a[2] + a[3], t3 = a[1] - a[4], t4 = a[2] - a[3];
    cf m1 = {a[0].x + c1 * t1.x + c2 * t2.x, a[0].y + c1 * t1.y + c2 * t2.y};
    cf m2 = {a[0].x + c2 * t1.x + c1 * t2.x, a[0].y + c2 * t1.y + c1 * t2.y};
    cf n1 = muli<DIR>(cf{s1 * t3.x + s2 * t4.x, s1 * t3.y + s2 * t4.y});
    cf n2 = muli<DIR>(cf{s2 * t3.x - s1 * t4.x, s2 * t3.y - s1 * t4.y});
    a[0] = a[0] + t1 + t2;
    a[1] = m1 + n1; a[4] = m1 - n1;
    a[2] = m2 + n2; a[3] = m2 - n2;
}
template <int DIR> DEV void fft10(cf (&a)[10]) {
    cf e[5], o[5];
#pragma unroll
    for (int n2 = 0; n2 < 5; ++n2) { const cf p = a[(2 * n2) % 10], q = a[(2 * n2 + 5) % 10]; e[n2] = p + q; o[n2] = p - q; }
    fft5<DIR>(e); fft5<DIR>(o);
#pragma unroll
    for (int k2 = 0; k2 < 5; ++k2) { a[(6 * k2) % 10] = e[k2]; a[(6 * k2 + 5) % 10] = o[k2]; }
}
template <int DIR> DEV void fft16_a(cf (&a)[16]) {
#pragma unroll
    for (int n2 = 0; n2 < 4; ++n2) fft4<DIR>(a[n2], a[n2 + 4], a[n2 + 8], a[n2 + 12]);
}
template <int DIR> DEV void fft16_t(cf (&a)[16]) {
    constexpr float C1 = 0.92387953251128674f, S1 = 0.38268343236508977f;
    const cf w1 = {C1, DIR * S1}, w3 = {S1, DIR * C1}, w9 = {-C1, -DIR * S1};
    a[1 + 4] = cmul(a[1 + 4], w1);
    a[1 + 8] = mulw8<DIR>(a[1 + 8]);
    a[1 + 12] = cmul(a[1 + 12], w3);
    a[2 + 4] = mulw8<DIR>(a[2 + 4]);
    a[2 + 8] = muli<DIR>(a[2 + 8]);
    a[2 + 12] = mulw83<DIR>(a[2 + 12]);
    a[3 + 4] = cmul(a[3 + 4], w3);
    a[3 + 8] = mulw83<DIR>(a[3 + 8]);
    a[3 + 12] = cmul(a[3 + 12], w9);
}
template <int DIR> DEV void fft16_b(cf (&a)[16]) {
#pragma unroll
    for (int k1 = 0; k1 < 4; ++k1) fft4<DIR>(a[4 * k1], a[4 * k1 + 1], a[4 * k1 + 2], a[4 * k1 + 3]);
    cf b[16];
#pragma unroll
    for (int k1 = 0; k1 < 4; ++k1)
#pragma unroll
        for (int k2 = 0; k2 < 4; ++k2) b[k1 + 4 * k2] = a[4 * k1 + k2];
#pragma unroll
    for (int i = 0; i < 16; ++i) a[i] = b[i];
}

template <int CTRL, int RM> DEV float dpp_add(float v) {
    int t = __builtin_amdgcn_update_dpp(0, __float_as_int(v), CTRL, RM, 0xf, true);
    return v + __int_as_float(t);
}
DEV float wave_sum(float v) {
    v = dpp_add<0x111, 0xf>(v);
    v = dpp_add<0x112, 0xf>(v);
    v = dpp_add<0x114, 0xf>(v);
    v = dpp_add<0x118, 0xf>(v);
    v = dpp_add<0x142, 0xa>(v);
    v = dpp_add<0x143, 0xc>(v);
    return __int_as_float(__builtin_amdgcn_readlane(__float_as_int(v), 63));
}
template <int CTRL, int RM> DEV unsigned dpp_max(unsigned v) {
    const unsigned t = (unsigned)__builtin_amdgcn_update_dpp(0, (int)v, CTRL, RM, 0xf, true);
    return v > t ? v : t;
}
DEV unsigned wave_max(unsigned v) {
    v = dpp_max<0x111, 0xf>(v); v = dpp_max<0x112, 0xf>(v); v = dpp_max<0x114, 0xf>(v); v = dpp_max<0x118, 0xf>(v);
    v = dpp_max<0x142, 0xa>(v); v = dpp_max<0x143, 0xc>(v);
    return (unsigned)__builtin_amdgcn_readlane((int)v, 63);
}
struct WMax { u32x4 a, b, c; };
DEV WMax vscale_load(const char* ws, int lane) { const u32x4* t = (const u32x4*)(ws + WS_WMAX) + lane; return {t[0], t[64], t[128]}; }
DEV int vscale_exp(const WMax& w) {
    const u32x4 a = w.a, b = w.b, c = w.c;
    unsigned m = a.x;
#pragma unroll
    for (int i = 0; i < 4; ++i) { m = m > a[i] ? m : a[i]; m = m > b[i] ? m : b[i]; m = m > c[i] ? m : c[i]; }
    int e = (int)(wave_max(m) >> 7) + 10;
    e = e < 16 ? 16 : e;
    return e > 238 ? 238 : e;
}
DEV float rdlane(float v, int l) { return __int_as_float(__builtin_amdgcn_readlane(__float_as_int(v), l)); }
#define PIN(x) asm volatile("" : "+v"(x))
#define SB() __builtin_amdgcn_sched_barrier(0)
DEV void bar() { SB(); __builtin_amdgcn_s_barrier(); asm volatile("" ::: "memory"); SB(); }
DEV void wait_lgkm0() { asm volatile("s_waitcnt lgkmcnt(0)" ::: "memory"); }
DEV void st16_wt(void* p, u32x4 v) { asm volatile("global_store_dwordx4 %0, %1, off sc1\n\ts_nop 1" ::"v"(p), "v"(v) : "memory"); }
DEV void st4_wt(void* p, unsigned v) { asm volatile("global_store_dword %0, %1, off sc1" ::"v"(p), "v"(v) : "memory"); }
DEV void st8_wt(void* p, f32x2 v) { asm volatile("global_store_dwordx2 %0, %1, off sc1" ::"v"(p), "v"(v) : "memory"); }
DEV int s0_of(int b) { return (b & 3) * 64 + ((b >> 2) & 1) * 32 + (b >> 3); }
#define CODE_ADDR(p, sym) do { const char* pc_; int off_; asm volatile("s_getpc_b64 %0\n\ts_mov_b32 %1, " #sym "@rel32@lo+4" : "=s"(pc_), "=s"(off_)); p = pc_ + off_; } while (0)
DEV void wave_fence() { asm volatile("" ::: "memory"); __builtin_amdgcn_wave_barrier(); }

DEV cf lds_ld(const char* base, int idx) { f32x2 v = *(const LAS f32x2*)((LAS const char*)base + idx * 8); return {v.x, v.y}; }
DEV void lds_st(char* base, int idx, cf a) { f32x2 v = {a.x, a.y}; *(LAS f32x2*)((LAS char*)base + idx * 8) = v; }

typedef f32x2 c2;
DEV c2 addi(c2 a, c2 b) { c2 r; asm("v_pk_add_f32 %0, %1, %2 op_sel:[0,1] op_sel_hi:[1,0] neg_lo:[0,1]" : "=v"(r) : "v"(a), "v"(b)); return r; }
DEV c2 subi(c2 a, c2 b) { c2 r; asm("v_pk_add_f32 %0, %1, %2 op_sel:[0,1] op_sel_hi:[1,0] neg_hi:[0,1]" : "=v"(r) : "v"(a), "v"(b)); return r; }
DEV c2 addc(c2 a, c2 b) { c2 r; asm("v_pk_add_f32 %0, %1, %2 neg_hi:[0,1]" : "=v"(r) : "v"(a), "v"(b)); return r; }
DEV c2 subc(c2 a, c2 b) { c2 r; asm("v_pk_add_f32 %0, %1, %2 neg_lo:[0,1]" : "=v"(r) : "v"(a), "v"(b)); return r; }
DEV c2 pmul(c2 a, c2 w) {
    c2 t = a.xx * w, r;
    asm("v_pk_fma_f32 %0, %1, %2, %3 op_sel:[1,1,0] op_sel_hi:[1,0,1] neg_lo:[0,1,0]" : "=v"(r) : "v"(a), "v"(w), "v"(t));
    return r;
}
DEV c2 pmulc(c2 a, c2 w) {
    c2 t = a.xx * w, r;
    asm("v_pk_fma_f32 %0, %1, %2, %3 op_sel:[1,1,0] op_sel_hi:[1,0,1] neg_hi:[0,1,0]" : "=v"(r) : "v"(a), "v"(w), "v"(t));
    return r;
}
DEV c2 pfma(c2 a, float k, c2 c) { return __builtin_elementwise_fma(a, c2{k, k}, c); }
DEV c2 ptw(float f) { c2 r = {__builtin_amdgcn_cosf(f), -__builtin_amdgcn_sinf(f)}; PIN(r); return r; }
DEV void pfft4(c2& a0, c2& a1, c2& a2, c2& a3) {
    c2 t0 = a0 + a2, t1 = a0 - a2, t2 = a1 + a3, t3 = a1 - a3;
    a0 = t0 + t2; a2 = t0 - t2; a1 = subi(t1, t3); a3 = addi(t1, t3);
}
DEV void pfft8(c2 (&a)[8]) {
    pfft4(a[0], a[2], a[4], a[6]);
    pfft4(a[1], a[3], a[5], a[7]);
    const c2 e0 = a[0], e1 = a[2], e2 = a[4], e3 = a[6], o0 = a[1], o2 = a[5];
    const c2 t1 = subi(a[3], a[3]), t3 = addi(a[7], a[7]);
    a[0] = e0 + o0; a[4] = e0 - o0;
    a[1] = pfma(t1, RH, e1); a[5] = pfma(t1, -RH, e1);
    a[2] = subi(e2, o2); a[6] = addi(e2, o2);
    a[3] = pfma(t3, -RH, e3); a[7] = pfma(t3, RH, e3);
}
DEV void pfft5(c2 (&a)[5]) {
    constexpr float c1 = 0.30901699437494742f, cc2 = -0.80901699437494742f;
    constexpr float s1 = 0.95105651629515357f, s2 = 0.58778525229247313f;
    const c2 t1 = a[1] + a[4], t2 = a[2] + a[3], t3 = a[1] - a[4], t4 = a[2] - a[3];
    const c2 m1 = pfma(t2, cc2, pfma(t1, c1, a[0])), m2 = pfma(t2, c1, pfma(t1, cc2, a[0]));
    const c2 n1 = pfma(t4, s2, t3 * s1), n2 = pfma(t4, -s1, t3 * s2);
    a[0] = a[0] + t1 + t2;
    a[1] = subi(m1, n1); a[4] = addi(m1, n1);
    a[2] = subi(m2, n2); a[3] = addi(m2, n2);
}
DEV void pfft10(c2 (&a)[10]) {
    c2 e[5], o[5];
#pragma unroll
    for (int n2 = 0; n2 < 5; ++n2) { const c2 p = a[(2 * n2) % 10], q = a[(2 * n2 + 5) % 10]; e[n2] = p + q; o[n2] = p - q; }
    pfft5(e); pfft5(o);
#pragma unroll
    for (int k2 = 0; k2 < 5; ++k2) { a[(6 * k2) % 10] = e[k2]; a[(6 * k2 + 5) % 10] = o[k2]; }
}
DEV c2 pld(const char* base, int idx) { return *(const LAS c2*)((LAS const char*)base + idx * 8); }
DEV void pst(char* base, int idx, c2 a) { *(LAS c2*)((LAS char*)base + idx * 8) = a; }
DEV unsigned pack_f16(c2 a) { return __builtin_bit_cast(unsigned, __builtin_convertvector(a, f16x2)); }

constexpr unsigned CANARY = 0x7f817f81u;
constexpr int RP_CODE = 6656, K12_CODE = 22784, K3_CODE = 10240;
constexpr int BUFA = 4 * 720 * 8, BUFB = 4 * 656 * 8, XP = 1296;
constexpr int WGRP = 3104, SLOT = 4 * WGRP, NS = 10, KSTEPS = 20;
constexpr int FSCR = 16 * 68 * 8;
#define X_WAIT 14
#define P1_WAIT 24
constexpr int L2_F = NS * SLOT, L2_FLAG = L2_F + 3 * FSCR;
constexpr int L2_CNT = L2_FLAG + 32, L2_PUB = L2_FLAG + 36, L2_GO = L2_FLAG + 40, L2_TAG = L2_FLAG + 44;
constexpr int L2_P1 = 6 * SLOT;
constexpr int L2_W2 = L2_FLAG + 64;
constexpr int K2_LDS = L2_W2 + 10240;

template <int N> DEV void wait_vm() { asm volatile("s_waitcnt vmcnt(%0)" ::"i"(N) : "memory"); }
DEV void touch(const char* p, unsigned lds) { asm volatile("s_mov_b32 m0, %1\n\ts_nop 0\n\tglobal_load_lds_dword %0, off" :: "v"(p), "s"(lds) : "memory"); }
DEV void sw_barrier(char* smem, int off, int lane, unsigned target) {
    wait_lgkm0();
    LAS unsigned* cnt = (LAS unsigned*)((LAS char*)smem + off);
    if (lane == 0) __hip_atomic_fetch_add(cnt, 1u, __ATOMIC_RELAXED, __HIP_MEMORY_SCOPE_WORKGROUP);
    while (__hip_atomic_load(cnt, __ATOMIC_RELAXED, __HIP_MEMORY_SCOPE_WORKGROUP) < target) {}
    asm volatile("" ::: "memory");
}
DEV void lds_inc(char* smem, int off, int lane) {
    if (lane == 0) __hip_atomic_fetch_add((LAS unsigned*)((LAS char*)smem + off), 1u, __ATOMIC_RELAXED, __HIP_MEMORY_SCOPE_WORKGROUP);
}
DEV void ln_row(const unsigned (&z)[5], char* dst, int lane) {
        float v[10];
#pragma unroll
        for (int i = 0; i < 5; ++i) { v[2 * i] = bf_lo(z[i]); v[2 * i + 1] = bf_hi(z[i]); }
        const float c0 = rdlane(v[0], 0);
        float s1 = 0.f, s2 = 0.f;
#pragma unroll
        for (int i = 0; i < 10; ++i) { float d = v[i] - c0; s1 += d; s2 += d * d; }
        s1 = wave_sum(s1); s2 = wave_sum(s2);
        const float md = s1 * (1.0f / 640.0f);
        float var = s2 * (1.0f / 640.0f) - md * md;
        var = var < 0.f ? 0.f : var;
        const float m = rbf(c0 + md);
        const float sd = rbf(__builtin_amdgcn_sqrtf(rbf(rbf(var) + 1e-5f)));
        const float rs = __builtin_amdgcn_rcpf(sd);
#pragma unroll
        for (int i = 0; i < 5; ++i) {
            unsigned d = pack_bf16(v[2 * i] - m, v[2 * i + 1] - m);
            *(LAS unsigned*)((LAS char*)dst + (lane + 64 * i) * 4) = pack_bf16(bf_lo(d) * rs, bf_hi(d) * rs);
        }
}
extern "C" __device__ __attribute__((noinline, visibility("hidden"))) void row_phase(const GAS unsigned* x32_, const GAS unsigned short* wr, const GAS unsigned short* wi, GAS char* ws_, unsigned lds_) {
    char* smem = (char*)(LAS char*)(uintptr_t)__builtin_amdgcn_readfirstlane(lds_);
    const int b = __builtin_amdgcn_workgroup_id_x(), tid = __builtin_amdgcn_workitem_id_x(), lane = tid & 63;
    const int wv = __builtin_amdgcn_readfirstlane(tid >> 6);
    char* ws = (char*)ws_;
    {
        const int s0 = s0_of(b);
        const int s = s0 + 256 * wv;
        unsigned z[5];
        const GAS unsigned* xp = x32_ + s * 320 + lane;
        asm volatile("global_load_dword %0, %5, off\n\tglobal_load_dword %1, %5, off offset:256\n\tglobal_load_dword %2, %5, off offset:512\n\t"
                     "global_load_dword %3, %5, off offset:768\n\tglobal_load_dword %4, %5, off offset:1024"
                     : "=&v"(z[0]), "=&v"(z[1]), "=&v"(z[2]), "=&v"(z[3]), "=&v"(z[4]) : "v"(xp) : "memory");
        const bool wtask = wv >= 1 && wv <= 3;
        const int task = b * 3 + wv - 1, id = task / 6, wq_ = id & 7, wa_ = id >> 3, k = (task - id * 6) * 64 + lane, kc = k < DF_ ? k : DF_ - 1;
        unsigned re[16], im[16];
        if (wtask) {
            const GAS unsigned short* pr = wr + (8 * wa_ + wq_) * DF_ + kc;
            const GAS unsigned short* pi = wi + (8 * wa_ + wq_) * DF_ + kc;
#pragma unroll
            for (int i = 0; i < 16; ++i)
                asm volatile("global_load_ushort %0, %2, off\n\tglobal_load_ushort %1, %3, off" : "=&v"(re[i]), "=&v"(im[i]) : "v"(pr + i * (128 * DF_)), "v"(pi + i * (128 * DF_)) : "memory");
        }
        const int qr = tid / 80, r = tid - qr * 80;
        c2 t640[8], t80[8];
#pragma unroll
        for (int a = 1; a < 8; ++a) t640[a] = ptw((float)(r * a) * (1.0f / 640.0f));
#pragma unroll
        for (int a = 1; a < 8; ++a) t80[a] = ptw((float)((r >> 3) * a) * (1.0f / 80.0f));
        const c2 twq = ptw((float)(s0 * (lane & 7)) * (1.0f / 2048.0f));
        SB();
        if (wtask) wait_vm<32>(); else wait_vm<0>();
        asm volatile("" : "+v"(z[0]), "+v"(z[1]), "+v"(z[2]), "+v"(z[3]), "+v"(z[4]));
        char* bufA = smem + L2_P1;
        char* bufB = bufA + BUFA;
        ln_row(z, bufB + wv * XP, lane);
        wait_lgkm0();
        bar();
        {
            c2 xr[8];
#pragma unroll
            for (int j = 0; j < 8; ++j) { const unsigned w = *(const LAS unsigned*)((LAS const char*)bufB + j * XP + tid * 4); xr[j] = c2{bf_lo(w), bf_hi(w)}; }
            const c2 a0 = xr[0] + xr[4], a1 = xr[0] - xr[4], b0 = xr[2] + xr[6], b1 = xr[2] - xr[6];
            const c2 e0 = xr[1] + xr[5], e1 = xr[1] - xr[5], d0 = xr[3] + xr[7], d1 = xr[3] - xr[7];
            const c2 p = a0 + b0, q = e0 + d0, u = e1 - d1, w = e1 + d1;
            const c2 y0 = p + q, y4 = p - q, y2r = a0 - b0, y2i = d0 - e0;
            const c2 y1r = pfma(u, RH, a1), y1i = pfma(w, -RH, -b1), y3r = pfma(u, -RH, a1), y3i = pfma(w, -RH, b1);
            *(LAS f32x4*)((LAS char*)bufA + (0 * 720 + 2 * tid) * 8) = f32x4{y0.x, y4.x, y0.y, y4.y};
            *(LAS f32x4*)((LAS char*)bufA + (1 * 720 + 2 * tid) * 8) = f32x4{y1r.x, y1i.x, y1r.y, y1i.y};
            *(LAS f32x4*)((LAS char*)bufA + (2 * 720 + 2 * tid) * 8) = f32x4{y2r.x, y2i.x, y2r.y, y2i.y};
            *(LAS f32x4*)((LAS char*)bufA + (3 * 720 + 2 * tid) * 8) = f32x4{y3r.x, y3i.x, y3r.y, y3i.y};
        }
        wait_lgkm0();
        bar();
        c2 a8[8];
        {
#pragma unroll
            for (int i = 0; i < 8; ++i) a8[i] = pld(bufA, qr * 720 + r + 80 * i);
            pfft8(a8);
            pst(bufB, qr * 656 + r, a8[0]);
#pragma unroll
            for (int a = 1; a < 8; ++a) pst(bufB, qr * 656 + a * 82 + r, pmul(a8[a], t640[a]));
        }
        sw_barrier(smem, L2_CNT, lane, 5);
        {
            const int ib = qr * 656 + (r & 7) * 82 + (r >> 3), ob = qr * 720 + (r >> 3) * 72 + (r & 7);
#pragma unroll
            for (int i = 0; i < 8; ++i) a8[i] = pld(bufB, ib + 10 * i);
            pfft8(a8);
            pst(bufA, ob, a8[0]);
#pragma unroll
            for (int a = 1; a < 8; ++a) pst(bufA, ob + 8 * a, pmul(a8[a], t80[a]));
        }
        sw_barrier(smem, L2_CNT, lane, 10);
        if (wv < 4) {
            c2 f[10];
#pragma unroll
            for (int t = 0; t < 10; ++t) f[t] = pld(bufA, wv * 720 + t * 72 + lane);
            pfft10(f);
            unsigned* T = (unsigned*)(ws + WS_T16) + (size_t)s0 * (8 * TP);
            if (wv == 0) {
#pragma unroll
                for (int bb = 5; bb < 10; ++bb) pst(bufB, lane + 64 * (bb - 5), f[bb]);
                if (lane == 0) pst(bufB, 320, f[0]);
                wave_fence();
                const c2 w4 = {rdlane(twq.y, 4) * 0.5f, rdlane(twq.x, 4) * -0.5f};
#pragma unroll
                for (int bb = 0; bb < 5; ++bb) {
                    const c2 h = pld(bufB, 320 - lane - 64 * bb);
                    st4_wt(T + (lane + 64 * bb), pack_f16(addc(f[bb], h) * 0.5f));
                    st4_wt(T + (4 * TP + lane + 64 * bb), pack_f16(pmul(subc(f[bb], h), w4)));
                }
                if (lane == 0) {
                    st4_wt(T + (320), pack_f16(c2{f[5].x, 0.f}));
                    st4_wt(T + (4 * TP + 320), pack_f16(pmul(subc(f[5], f[5]), w4)));
                }
            } else {
                const c2 wa = {rdlane(twq.x, wv), rdlane(twq.y, wv)}, wb = {rdlane(twq.x, 8 - wv), rdlane(twq.y, 8 - wv)};
                unsigned* Ta = T + wv * TP;
                unsigned* Tb = T + (8 - wv) * TP;
#pragma unroll
                for (int bb = 0; bb < 5; ++bb) st4_wt(Ta + (lane + 64 * bb), pack_f16(pmul(f[bb], wa)));
#pragma unroll
                for (int bb = 5; bb < 10; ++bb) st4_wt(Tb + (640 - lane - 64 * bb), pack_f16(pmulc(f[bb], wb)));
                if (lane == 0) {
                    st4_wt(Tb + (0), pack_f16(pmulc(f[0], wb)));
                    st4_wt(Ta + (320), pack_f16(pmul(f[5], wa)));
                }
                wait_vm<12>();
                unsigned o[16], mx = 0u;
#pragma unroll
                for (int i = 0; i < 16; ++i) {
                    asm volatile("" : "+v"(re[i]), "+v"(im[i]));
                    o[i] = re[i] | (im[i] << 16);
                    const unsigned ma = re[i] & 0x7fffu, mb = im[i] & 0x7fffu;
                    mx = mx > ma ? mx : ma; mx = mx > mb ? mx : mb;
                }
                mx = wave_max(mx);
                if (lane == 0) st4_wt((unsigned*)(ws + WS_WMAX) + task, mx);
                if (k < 4 * NG4) {
                    u32x4* Wt = (u32x4*)(ws + WS_WT) + (wq_ * NG4 + (k >> 2)) * 256 + wa_ * 4 + (k & 3);
#pragma unroll
                    for (int bq = 0; bq < 4; ++bq) st16_wt(Wt + bq * 64, u32x4{o[4 * bq], o[4 * bq + 1], o[4 * bq + 2], o[4 * bq + 3]});
                }
            }
        }

    }
}

DEV unsigned lds_min4(const char* smem, int off) {
    u32x4 c = *(const volatile LAS u32x4*)((LAS const char*)smem + off);
    unsigned a = c.x < c.y ? c.x : c.y, b = c.z < c.w ? c.z : c.w;
    return __builtin_amdgcn_readfirstlane(a < b ? a : b);
}
DEV unsigned lds_word(const char* smem, int off) { return __builtin_amdgcn_readfirstlane(*(const volatile LAS unsigned*)((LAS const char*)smem + off)); }
struct Dma { unsigned wo[3]; const char* w1b; const char* w3b; unsigned lw; char* smem; char* ws; };
constexpr int EARLY = 6;
constexpr int issued_before(int p) { int n = p < EARLY ? EARLY : p + 3; return n > KSTEPS ? KSTEPS : n; }
template <int ST> DEV void dma_stage(const Dma& d) {
    if constexpr (ST >= NS) {
        while (lds_min4(d.smem, L2_FLAG + 16) < (unsigned)(ST - NS + 1)) __builtin_amdgcn_s_sleep(1);
    }
#pragma unroll
    for (int ks = 0; ks < 2; ++ks) {
        const char* g1 = d.w1b + (size_t)(2 * ST + ks) * (16 * F_ * 2);
        const char* g3 = d.w3b + (size_t)(2 * ST + ks) * (16 * F_ * 2);
        const unsigned l0 = d.lw + (ST % NS) * SLOT + ks * (2 * WGRP), l1 = l0 + WGRP;
        asm volatile("s_mov_b32 m0, %5\n\ts_nop 0\n\t"
                     "global_load_lds_dwordx4 %0, %3\n\t"
                     "global_load_lds_dwordx4 %1, %3 offset:1024\n\t"
                     "global_load_lds_dwordx4 %2, %3 offset:2048\n\t"
                     "s_mov_b32 m0, %6\n\ts_nop 0\n\t"
                     "global_load_lds_dwordx4 %0, %4\n\t"
                     "global_load_lds_dwordx4 %1, %4 offset:1024\n\t"
                     "global_load_lds_dwordx4 %2, %4 offset:2048"
                     :: "v"(d.wo[0]), "v"(d.wo[1]), "v"(d.wo[2]), "s"(g1), "s"(g3), "s"(l0), "s"(l1) : "memory");
    }
}
template <int A, int B> DEV void dma_range(const Dma& d) { if constexpr (A < B) { dma_stage<A>(d); dma_range<A + 1, B>(d); } }
template <int P> DEV void dma_run(const Dma& d) {
    if constexpr (P < KSTEPS) {
        if constexpr (P > 0) dma_range<issued_before(P - 1), issued_before(P)>(d);
        wait_vm<12 * (issued_before(P) - 1 - P)>();
        *(volatile LAS unsigned*)((LAS char*)d.smem + L2_FLAG) = (unsigned)(P + 1);
        dma_run<P + 1>(d);
    }
}

DEV float silu_f(float v) { return v * __builtin_amdgcn_rcpf(1.0f + __builtin_amdgcn_exp2f(v * -1.44269504088896341f)); }
DEV u32x4 wfrag(const char* smem, unsigned a) {
    s16x4 lo = __builtin_amdgcn_ds_read_tr16_b64_v4i16((LAS s16x4*)((LAS char*)smem + a));
    s16x4 hi = __builtin_amdgcn_ds_read_tr16_b64_v4i16((LAS s16x4*)((LAS char*)smem + a + 768));
    return __builtin_bit_cast(u32x4, __builtin_shufflevector(lo, hi, 0, 1, 2, 3, 4, 5, 6, 7));
}

extern "C" __global__ NOPK __launch_bounds__(512) void k12_kernel(const unsigned* __restrict__ x32, const char* __restrict__ w1, const char* __restrict__ w3,
                                                        const char* __restrict__ w2, const unsigned short* __restrict__ wr,
                                                        const unsigned short* __restrict__ wi, char* __restrict__ ws) {
    extern __shared__ __attribute__((aligned(16))) char smem[];
    const int b = blockIdx.x, tid = threadIdx.x, lane = tid & 63;
    const int wv = __builtin_amdgcn_readfirstlane(tid >> 6);
    const int xcd = b & 7, idx = b >> 3;
    const int mt = (xcd & 3) * 4 + (idx & 3), nt = (xcd >> 2) * 8 + (idx >> 2);
    STAMP_DECL;
    STAMP(1, 0);
    Dma d;
#pragma unroll
    for (int r = 0; r < 3; ++r) {
        const int L = r * 64 + lane, row = L / 12, c = L - row * 12;
        d.wo[r] = (unsigned)(row * (F_ * 2) + c * 16 - r * 1024);
    }
    d.smem = smem; d.ws = ws;
    d.w1b = w1 + nt * 192; d.w3b = w3 + nt * 192;
    d.lw = (unsigned)(uintptr_t)(LAS char*)smem;
    unsigned* sync = (unsigned*)(ws + WS_SYNC);
    if (wv < 5) {
        unsigned lb = d.lw;
        PIN(lb);
        row_phase((const GAS unsigned*)x32, (const GAS unsigned short*)wr, (const GAS unsigned short*)wi, (GAS char*)ws, lb);
    } else {
        const int s0 = s0_of(b);
        const unsigned* xp = x32 + (s0 + 256 * wv) * 320 + lane;
        unsigned z[5], nonce;
        asm volatile("global_load_dword %0, %6, off\n\tglobal_load_dword %1, %6, off offset:256\n\tglobal_load_dword %2, %6, off offset:512\n\t"
                     "global_load_dword %3, %6, off offset:768\n\tglobal_load_dword %4, %6, off offset:1024\n\tglobal_load_dword %5, %7, off sc1"
                     : "=&v"(z[0]), "=&v"(z[1]), "=&v"(z[2]), "=&v"(z[3]), "=&v"(z[4]), "=&v"(nonce) : "v"(xp), "v"(sync) : "memory");
        {
            const char *pc, *pr; CODE_ADDR(pc, k12_kernel); CODE_ADDR(pr, row_phase);
            const int L = tid - 320;
            touch(pr + (L < RP_CODE / 128 ? L : 0) * 128, d.lw + L2_W2);
            touch(pc + (L < K12_CODE / 128 ? L : 0) * 128, d.lw + L2_W2);
        }
        if (wv == 5) dma_range<0, 1>(d); else if (wv == 6) dma_range<1, 2>(d); else dma_range<2, 3>(d);
        asm volatile("s_waitcnt vmcnt(14)" : "+v"(z[0]), "+v"(z[1]), "+v"(z[2]), "+v"(z[3]), "+v"(z[4]), "+v"(nonce) :: "memory");
        char* bufB = smem + L2_P1 + BUFA;
        if (wv == 6 && lane < 12) *(volatile LAS unsigned*)((LAS char*)smem + L2_FLAG + lane * 4) = lane == 0 ? (unsigned)EARLY : lane == 11 ? nonce + 1u : 0u;
        ln_row(z, bufB + wv * XP, lane);
        wait_lgkm0();
        bar();
        STAMP(1, 5);
        u32x4 pc[4];
#pragma unroll
        for (int i = 0; i < 4; ++i) { const int p = tid - 320 + 192 * i, pp = p < 640 ? p : 0; pc[i] = *(const LAS u32x4*)((LAS const char*)bufB + (pp & 7) * XP + (pp >> 3) * 16); }
        const unsigned tag = lds_word(smem, L2_TAG);
        wait_lgkm0();
        char* xt = ws + WS_XN + (size_t)(s0 >> 4) * (20 * 8192) + ((s0 >> 2) & 3) * 1024 + (s0 & 3) * 128;
#pragma unroll
        for (int i = 0; i < 4; ++i) {
            const int p = tid - 320 + 192 * i, c = p >> 3;
            if (p < 640) st16_wt(xt + (c >> 1) * 4096 + (c & 1) * 512 + (p & 7) * 16, pc[i]);
        }
        bar();
        if (wv == 5) dma_range<3, 4>(d); else if (wv == 6) dma_range<4, 5>(d); else dma_range<5, 6>(d);
        wait_vm<12>();
        if (lane == 0) st4_wt(sync + 1024 + s0 * 4 + (wv - 5), tag);
        {
            constexpr int R0 = 32 * EARLY, NA = (D_ - R0) * 24, NT = 2 * NA + 15360, PER = NT / 768;
            static_assert(NT % 768 == 0 && PER <= 64, "");
            const int L = (b * 3 + wv - 5) * PER + (lane < PER ? lane : 0);
            const char* p = L < NA ? w1 + (size_t)(R0 * 24 + L) * 128 : L < 2 * NA ? w3 + (size_t)(R0 * 24 + L - NA) * 128 : w2 + (size_t)(L - 2 * NA) * 128;
            const char* p3; CODE_ADDR(p3, k3_kernel);
            const int Lc = (idx & 7) * 48 + (wv - 5) * 16 + lane - PER;
            if (lane >= PER && Lc < K3_CODE / 128) p = p3 + Lc * 128;
            touch(p, d.lw + L2_W2);
        }
        STAMP(1, 6);
    }
    STAMP(1, 7);
    if (wv == 0) {
        __builtin_amdgcn_s_setprio(2);
        wait_lgkm0();
        lds_inc(smem, L2_FLAG + 8, lane);
        wait_vm<0>();
        lds_inc(smem, L2_PUB, lane);
        while (lds_word(smem, L2_FLAG + 8) < 7u) __builtin_amdgcn_s_sleep(1);
        STAMP(1, 1);
        dma_range<EARLY, issued_before(EARLY)>(d);
        wait_vm<12 * (issued_before(EARLY) - 1 - EARLY)>();
        *(volatile LAS unsigned*)((LAS char*)smem + L2_FLAG) = (unsigned)(EARLY + 1);
        dma_run<EARLY + 1>(d);
        STAMP(1, 11);
        unsigned wo[5];
#pragma unroll
        for (int r = 0; r < 5; ++r) {
            const int L = r * 64 + lane, row = L / 10, c = L - row * 10;
            wo[r] = (unsigned)(row * (D_ * 2) + c * 16 - (r & 3) * 1024);
        }
        const int nu = b < 128 ? 2 : 1;
        for (int i = 0; i < nu; ++i) {
            const int u = b + 256 * i, nt2 = u / 48, kb = u - nt2 * 48;
            const char* g = w2 + (size_t)kb * (32 * D_ * 2) + nt2 * 160;
            const unsigned l0 = d.lw + L2_W2 + i * 5120, l1 = l0 + 4096;
            asm volatile("s_mov_b32 m0, %6\n\ts_nop 0\n\t"
                         "global_load_lds_dwordx4 %0, %5\n\t"
                         "global_load_lds_dwordx4 %1, %5 offset:1024\n\t"
                         "global_load_lds_dwordx4 %2, %5 offset:2048\n\t"
                         "global_load_lds_dwordx4 %3, %5 offset:3072\n\t"
                         "s_mov_b32 m0, %7\n\ts_nop 0\n\t"
                         "global_load_lds_dwordx4 %4, %5"
                         :: "v"(wo[0]), "v"(wo[1]), "v"(wo[2]), "v"(wo[3]), "v"(wo[4]), "s"(g), "s"(l0), "s"(l1) : "memory");
        }
        wait_vm<0>();
        const int gq = lane >> 4;
        const unsigned ra = L2_W2 + (16 * (gq >> 1) + 4 * (gq & 1) + ((lane & 15) >> 2)) * 160 + (lane & 3) * 8;
        for (int i = 0; i < nu; ++i) {
            char* dst = ws + WS_W2T + (size_t)(b + 256 * i) * 5120 + lane * 16;
#pragma unroll
            for (int j = 0; j < 5; ++j) {
                s16x4 lo = __builtin_amdgcn_ds_read_tr16_b64_v4i16((LAS s16x4*)((LAS char*)smem + ra + i * 5120 + j * 32));
                s16x4 hi = __builtin_amdgcn_ds_read_tr16_b64_v4i16((LAS s16x4*)((LAS char*)smem + ra + i * 5120 + j * 32 + 8 * 160));
                st16_wt(dst + j * 1024, __builtin_bit_cast(u32x4, __builtin_shufflevector(lo, hi, 0, 1, 2, 3, 4, 5, 6, 7)));
            }
        }
        STAMP(1, 12); STAMP_FLUSH(1);
    } else if (wv >= 4) {
        __builtin_amdgcn_s_setprio(3);
        const int mw = wv - 4;
        const int h = lane >> 5, r32 = lane & 31;
        const int l16 = lane * 16;
        const __amdgpu_buffer_rsrc_t xr = __builtin_amdgcn_make_buffer_rsrc((void*)(ws + WS_XN + (size_t)mt * (20 * 8192) + mw * 1024), 0, 20 * 8192, 0x00020000);
        if (wv == 4) {
            const unsigned tag = lds_word(smem, L2_TAG);
            const unsigned* tp = sync + 1024 + mt * 64 + lane;
            __builtin_amdgcn_s_sleep(X_WAIT);
            for (unsigned spin = 0; spin < (1u << 20); ++spin) {
                unsigned f;
                asm volatile("global_load_dword %0, %1, off sc1\n\ts_waitcnt vmcnt(0)" : "=&v"(f) : "v"(tp) : "memory");
                if (__builtin_amdgcn_ballot_w64(f != tag && (lane & 3) != 3) == 0ull) break;
                __builtin_amdgcn_s_sleep(10);
            }
            *(volatile LAS unsigned*)((LAS char*)smem + L2_FLAG + 4) = 1u;
        } else {
            wait_vm<0>();
            lds_inc(smem, L2_FLAG + 8, lane);
            while (lds_word(smem, L2_FLAG + 4) == 0u) __builtin_amdgcn_s_sleep(1);
        }
        STAMP(1, 8);
        u32x4 xf[5][2];
#pragma unroll
        for (int st = 0; st < 5; ++st) {
            xf[st][0] = __builtin_amdgcn_raw_buffer_load_b128(xr, l16, st * 8192, 16);
            xf[st][1] = __builtin_amdgcn_raw_buffer_load_b128(xr, l16, st * 8192 + 4096, 16);
        }
        f32x16 acc[6];
#pragma unroll
        for (int i = 0; i < 6; ++i)
#pragma unroll
            for (int r = 0; r < 16; ++r) acc[i][r] = 0.f;
        u32x4 wa[6], wb[6];
        unsigned rc = ((lane >> 4) & 1) * WGRP + (8 * h + ((lane & 15) >> 2)) * 192 + (lane & 3) * 8, rn = rc + 5 * SLOT;
        unsigned fa = L2_FLAG, da = L2_FLAG + 16 + mw * 4, dv = 0;
        PIN(rc); PIN(rn); PIN(fa); PIN(da); PIN(dv);
#define MM(i, w, x) asm volatile("v_mfma_f32_32x32x16_bf16 %0, %1, %2, %0" : "+v"(acc[i]) : "v"(w), "v"(x) : "memory")
        while (lds_word(smem, L2_FLAG + 8) < 7u) __builtin_amdgcn_s_sleep(1);
        STAMP(1, 1);
#pragma unroll
        for (int i = 0; i < 6; ++i) wa[i] = wfrag(smem, rc + i * 32);
#pragma unroll 1
        for (int io = 0; io < 3; ++io) {
#pragma unroll
            for (int ii = 0; ii < 5; ++ii) {
                const int it = io * 5 + ii;
                const unsigned cb = rc + ii * SLOT + 2 * WGRP, nb = ii < 4 ? rc + (ii + 1) * SLOT : rn;
                MM(0, wa[0], xf[ii][0]);
                wb[0] = wfrag(smem, cb);
                unsigned fl = *(const volatile LAS unsigned*)((LAS const char*)smem + fa);
                MM(1, wa[1], xf[ii][0]);
                wb[1] = wfrag(smem, cb + 32);
                MM(2, wa[2], xf[ii][0]);
                wb[2] = wfrag(smem, cb + 64);
                MM(3, wa[3], xf[ii][0]);
                wb[3] = wfrag(smem, cb + 96);
                asm volatile("v_add_u32 %0, 1, %0" : "+v"(dv));
                MM(4, wa[4], xf[ii][0]);
                wb[4] = wfrag(smem, cb + 128);
                {
                    unsigned m = __builtin_amdgcn_readfirstlane(fl);
                    const unsigned need = it == 14 ? KSTEPS : it + 2;
                    while (m < need) { __builtin_amdgcn_s_sleep(1); m = lds_word(smem, L2_FLAG); }
                }
                MM(5, wa[5], xf[ii][0]);
                wb[5] = wfrag(smem, cb + 160);
                MM(0, wb[0], xf[ii][1]);
                wa[0] = wfrag(smem, nb);
                xf[ii][0] = __builtin_amdgcn_raw_buffer_load_b128(xr, l16, (it + 5) * 8192, 16);
                MM(1, wb[1], xf[ii][1]);
                wa[1] = wfrag(smem, nb + 32);
                MM(2, wb[2], xf[ii][1]);
                wa[2] = wfrag(smem, nb + 64);
                MM(3, wb[3], xf[ii][1]);
                wa[3] = wfrag(smem, nb + 96);
                MM(4, wb[4], xf[ii][1]);
                wa[4] = wfrag(smem, nb + 128);
                MM(5, wb[5], xf[ii][1]);
                wa[5] = wfrag(smem, nb + 160);
                if ((it & 1) && io < 2) *(volatile LAS unsigned*)((LAS char*)smem + da) = dv;
                xf[ii][1] = __builtin_amdgcn_raw_buffer_load_b128(xr, l16, (it + 5) * 8192 + 4096, 16);
            }
            { const unsigned t = rc; rc = rn; rn = t; }
            STAMP(1, 2 + io); CSTAMP(1, 2 + io);
        }
        char* ht = ws + WS_HT + (size_t)((((mt * 2 + (mw >> 1)) * 48 + 3 * nt) * 4 + 2 * (mw & 1) + (r32 >> 4)) * 64 + (r32 & 15) + 16 * h) * 16;
        u32x4 g0[9], g1[9];
#define TFRAG(i, n) wfrag(smem, rc + ((n) >> 1) * SLOT + ((n) & 1) * (2 * WGRP) + (i) * 32)
#pragma unroll
        for (int n = 1; n < 10; ++n) g0[n - 1] = TFRAG(0, n);
#pragma unroll
        for (int i = 0; i < 7; ++i) {
            float hh[8];
#pragma unroll
            for (int n = 0; n < 10; ++n) {
                if (i < 6) {
                    if (n == 0) MM(i, wa[i], xf[0][0]);
                    else if (i & 1) MM(i, g1[n - 1], xf[n >> 1][n & 1]);
                    else MM(i, g0[n - 1], xf[n >> 1][n & 1]);
                    if (i < 5 && n < 9) { if (i & 1) g0[n] = TFRAG(i + 1, n + 1); else g1[n] = TFRAG(i + 1, n + 1); }
                } else if (n == 0) {
                    asm volatile("s_nop 15\n\ts_nop 7" ::: "memory");
                }
                if (i > 0) {
                    if (n >= 1 && n <= 8) { hh[n - 1] = silu_f(acc[i - 1][n - 1]) * acc[i - 1][8 + n - 1]; PIN(hh[n - 1]); }
                    if (n == 9) {
                        const u32x4 o = {pack_bf16(hh[0], hh[1]), pack_bf16(hh[2], hh[3]), pack_bf16(hh[4], hh[5]), pack_bf16(hh[6], hh[7])};
                        st16_wt(ht + ((i - 1) >> 1) * 4096 + ((i - 1) & 1) * 512, o);
                    }
                }
                SB();
            }
        }
        STAMP(1, 12); STAMP_FLUSH(1);
    } else {
        const int fw = wv - 1;
        const int q = xcd, g4 = fw * 32 + idx;
        wait_lgkm0();
        lds_inc(smem, L2_FLAG + 8, lane);
        wait_vm<0>();
        lds_inc(smem, L2_PUB, lane);
        if (g4 >= NG4) return;
        if (fw == 0) {
            const unsigned tag = lds_word(smem, L2_TAG);
            while (lds_word(smem, L2_PUB) < 4u) __builtin_amdgcn_s_sleep(1);
            if (lane < 8) st4_wt(sync + 2048 + lane * 256 + b, tag);
            STAMP(1, 9);
        }
        const int t = lane >> 2;
        cf tw[16];
#pragma unroll
        for (int a = 1; a < 16; ++a) { tw[a] = twid((float)(t * a) * (1.0f / 256.0f)); PIN(tw[a].x); PIN(tw[a].y); }
        SB();
        if (fw == 0) {
            const unsigned tag = lds_word(smem, L2_TAG);
            __builtin_amdgcn_s_sleep(P1_WAIT);
            const unsigned* tp = sync + 2048 + xcd * 256 + lane * 4;
            for (unsigned spin = 0; spin < (1u << 20); ++spin) {
                u32x4 f;
                asm volatile("global_load_dwordx4 %0, %1, off sc1\n\ts_waitcnt vmcnt(0)" : "=&v"(f) : "v"(tp) : "memory");
                if (__builtin_amdgcn_ballot_w64(f.x != tag || f.y != tag || f.z != tag || f.w != tag) == 0ull) break;
                __builtin_amdgcn_s_sleep(16);
            }
            *(volatile LAS unsigned*)((LAS char*)smem + L2_GO) = 1u;
            if (b == 0 && lane == 0) st4_wt(sync, tag);
        } else {
            while (lds_word(smem, L2_GO) == 0u) __builtin_amdgcn_s_sleep(4);
        }
        STAMP(1, 10);
        const int eo = (t * 8 + q) * TP + 4 * g4 + (lane & 3);
        const __amdgpu_buffer_rsrc_t tr = __builtin_amdgcn_make_buffer_rsrc((void*)(ws + WS_T16), 0, 2048 * TP * 4, 0x00020000);
        const __amdgpu_buffer_rsrc_t vr = __builtin_amdgcn_make_buffer_rsrc((void*)(ws + WS_T), 0, 2048 * TP * 4, 0x00020000);
        const __amdgpu_buffer_rsrc_t wtr = __builtin_amdgcn_make_buffer_rsrc((void*)(ws + WS_WT + (size_t)(q * NG4 + g4) * 4096), 0, 4096, 0x00020000);
        const __amdgpu_buffer_rsrc_t wmr = __builtin_amdgcn_make_buffer_rsrc((void*)(ws + WS_WMAX), 0, 3072, 0x00020000);
        unsigned tv[16];
        u32x4 wq[4];
#pragma unroll
        for (int i = 0; i < 16; ++i) tv[i] = __builtin_amdgcn_raw_buffer_load_b32(tr, eo * 4, i * (128 * TP * 4), 16);
#pragma unroll
        for (int i = 0; i < 4; ++i) wq[i] = __builtin_amdgcn_raw_buffer_load_b128(wtr, lane * 16, i * 1024, 16);
        const WMax wm = {__builtin_amdgcn_raw_buffer_load_b128(wmr, lane * 16, 0, 16), __builtin_amdgcn_raw_buffer_load_b128(wmr, lane * 16, 1024, 16), __builtin_amdgcn_raw_buffer_load_b128(wmr, lane * 16, 2048, 16)};
        SB();
        char* fs = smem + L2_F + fw * FSCR;
        const float vs = __builtin_bit_cast(float, (unsigned)(254 - vscale_exp(wm)) << 23);
        cf v[16];
#pragma unroll
        for (int i = 0; i < 16; ++i) { const f16x2 hv = __builtin_bit_cast(f16x2, tv[i]); v[i] = {(float)hv.x, (float)hv.y}; }
        STAMP(1, 1);
        fft16_a<-1>(v); fft16_t<-1>(v); fft16_b<-1>(v);
        lds_st(fs, lane, v[0]);
#pragma unroll
        for (int a = 1; a < 16; ++a) lds_st(fs, a * 68 + lane, cmul(v[a], tw[a]));
        wave_fence();
#pragma unroll
        for (int i = 0; i < 16; ++i) v[i] = lds_ld(fs, t * 68 + i * 4 + (lane & 3));
        fft16_a<-1>(v); fft16_t<-1>(v); fft16_b<-1>(v);
#pragma unroll
        for (int i = 0; i < 16; ++i) { const unsigned w = wq[i >> 2][i & 3]; v[i] = cmul(v[i], cf{bf_lo(w), bf_hi(w)}); }
        if (fw < 2) {
            char* sp = ws + WS_S + (size_t)b * 10240 + fw * 5120 + lane * 16;
#pragma unroll
            for (int i = 0; i < 5; ++i) st16_wt(sp + i * 1024, u32x4{CANARY, CANARY, CANARY, CANARY});
        }
        fft16_a<1>(v); fft16_t<1>(v); fft16_b<1>(v);
        wave_fence();
        lds_st(fs, t * 68 + (lane & 3), v[0]);
#pragma unroll
        for (int i = 1; i < 16; ++i) lds_st(fs, t * 68 + i * 4 + (lane & 3), cmulc(v[i], tw[i]));
        wave_fence();
#pragma unroll
        for (int a = 0; a < 16; ++a) v[a] = lds_ld(fs, a * 68 + lane);
        fft16_a<1>(v); fft16_t<1>(v); fft16_b<1>(v);
#pragma unroll
        for (int i = 0; i < 16; ++i)
            __builtin_amdgcn_raw_buffer_store_b32(__builtin_bit_cast(unsigned, f16x2{(_Float16)(v[i].x * vs), (_Float16)(v[i].y * vs)}), vr, eo * 4, i * (128 * TP * 4), 0);
        STAMP(1, 12); STAMP_FLUSH(1);
    }
}

constexpr int RPITCH = 336;
constexpr int L3_RED = 0;
constexpr int L3_A = 4 * 64 * RPITCH;
constexpr int L3_B = L3_A + 4 * 720 * 8;
constexpr int L3_CNT = L3_B + 4 * 656 * 8;
constexpr int K3_LDS = L3_CNT + 64;
#define GO_STEP 9

DEV void helper_barrier(char* smem, int lane, unsigned target) {
    wait_lgkm0();
    LAS unsigned* cnt = (LAS unsigned*)((LAS char*)smem + L3_CNT);
    if (lane == 0) __hip_atomic_fetch_add(cnt, 1u, __ATOMIC_RELAXED, __HIP_MEMORY_SCOPE_WORKGROUP);
    while (__hip_atomic_load(cnt, __ATOMIC_RELAXED, __HIP_MEMORY_SCOPE_WORKGROUP) < target) __builtin_amdgcn_s_sleep(1);
    asm volatile("" ::: "memory");
}
#define MFMA16(a, b, c) __builtin_amdgcn_mfma_f32_16x16x32_bf16(__builtin_bit_cast(bf16x8, a), __builtin_bit_cast(bf16x8, b), c, 0, 0, 0)

extern "C" __global__ NOPK __launch_bounds__(768) void k3_kernel(const char* __restrict__ x, char* __restrict__ ws, char* __restrict__ out) {
    extern __shared__ __attribute__((aligned(16))) char smem[];
    const int b = blockIdx.x, tid = threadIdx.x, lane = tid & 63;
    const int wv = __builtin_amdgcn_readfirstlane(tid >> 6);
    const int xcd = b & 7, idx = b >> 3;
    const int mt2 = (xcd & 3) * 8 + (idx & 7), nt2 = (xcd >> 2) * 4 + (idx >> 3);
    STAMP_DECL;
    STAMP(2, 0);
    if (tid < 8) *(LAS unsigned*)((LAS char*)smem + L3_CNT + tid * 4) = 0u;
    wait_lgkm0();
    bar();
    if (wv >= 8) {
        __builtin_amdgcn_s_setprio(2);
        const int gw = wv - 8;
        const char* hb = ws + WS_HT + (size_t)(mt2 * 48 + 12 * gw) * 4096;
        const char* wb = ws + WS_W2T + (size_t)(nt2 * 48 + 12 * gw) * 5120;
        const int l16 = lane * 16;
        const __amdgpu_buffer_rsrc_t hr = __builtin_amdgcn_make_buffer_rsrc((void*)hb, 0, 12 * 4096, 0x00020000);
        const __amdgpu_buffer_rsrc_t wr_ = __builtin_amdgcn_make_buffer_rsrc((void*)wb, 0, 12 * 5120, 0x00020000);
#define HLD(st, tb) __builtin_amdgcn_raw_buffer_load_b128(hr, l16 + (tb) * 1024, (st) * 4096, 0)
#define WLD(st, j) __builtin_amdgcn_raw_buffer_load_b128(wr_, l16 + ((j) & 3) * 1024, (st) * 5120 + ((j) >> 2) * 4096, 0)
        CSTAMP(2, 0);
        u32x4 hf[2][4], wf[2][5];
#pragma unroll
        for (int st = 0; st < 2; ++st) {
#pragma unroll
            for (int tb = 0; tb < 4; ++tb) hf[st][tb] = HLD(st, tb);
#pragma unroll
            for (int j = 0; j < 5; ++j) wf[st][j] = WLD(st, j);
            SB();
        }
        f32x4 acc[5][4];
#pragma unroll
        for (int j = 0; j < 5; ++j)
#pragma unroll
            for (int tb = 0; tb < 4; ++tb) acc[j][tb] = f32x4{0.f, 0.f, 0.f, 0.f};
        char* rp = smem + L3_RED + gw * (64 * RPITCH) + (lane & 15) * RPITCH + (lane >> 4) * 16;
        SB();
#pragma unroll
        for (int st = 0; st < 12; ++st) {
            const int sl = st & 1;
            const bool more = st + 2 < 12;
#pragma unroll
            for (int j = 0; j < 4; ++j) {
#pragma unroll
                for (int tb = 0; tb < 4; ++tb) acc[j][tb] = MFMA16(wf[sl][j], hf[sl][tb], acc[j][tb]);
                SB();
                if (more) wf[sl][j] = WLD(st + 2, j);
                if (st == 11 && j > 0) {
#pragma unroll
                    for (int tb = 0; tb < 4; ++tb) *(LAS f32x4*)((LAS char*)rp + tb * (16 * RPITCH) + (j - 1) * 64) = acc[j - 1][tb];
                }
                SB();
            }
#pragma unroll
            for (int tb = 0; tb < 4; ++tb) {
                acc[4][tb] = MFMA16(wf[sl][4], hf[sl][tb], acc[4][tb]);
                SB();
                if (more) hf[sl][tb] = HLD(st + 2, tb);
                SB();
            }
            if (more) wf[sl][4] = WLD(st + 2, 4);
            if (st == GO_STEP) *(volatile LAS unsigned*)((LAS char*)smem + L3_CNT + 16 + gw * 4) = 1u;
            SB();
        }
        STAMP(2, 1); CSTAMP(2, 1);
#pragma unroll
        for (int j = 3; j < 5; ++j)
#pragma unroll
            for (int tb = 0; tb < 4; ++tb) *(LAS f32x4*)((LAS char*)rp + tb * (16 * RPITCH) + j * 64) = acc[j][tb];
        wait_lgkm0();
        STAMP(2, 2);
        bar();
        STAMP_FLUSH(2);
    } else {
        __builtin_amdgcn_s_setprio(3);
        const int s0 = s0_of(b);
        const f16x2* V = (const f16x2*)(ws + WS_T) + (size_t)s0 * (8 * TP);
        char* sg = ws + WS_S;
        f16x2 u[10], u2[10], e0, e1;
        const WMax wm = vscale_load(ws, lane);
        if (wv < 4) {
            const int qa = wv, qb = wv == 0 ? 0 : 8 - wv;
#pragma unroll
            for (int bb = 0; bb < 5; ++bb) u[bb] = V[qa * TP + lane + 64 * bb];
#pragma unroll
            for (int bb = 5; bb < 10; ++bb) u[bb] = V[qb * TP + 640 - lane - 64 * bb];
            if (wv == 0) {
#pragma unroll
                for (int bb = 0; bb < 5; ++bb) u2[bb] = V[4 * TP + lane + 64 * bb];
#pragma unroll
                for (int bb = 5; bb < 10; ++bb) u2[bb] = V[4 * TP + 640 - lane - 64 * bb];
            } else {
                e0 = V[qb * TP];
                e1 = V[qa * TP + 320];
            }
        }
        SB();
        __builtin_amdgcn_s_setprio(0);
        STAMP(2, 12);
        if (wv < 5) {
            const int qr = tid / 80, r = tid - qr * 80;
            cf t80[10], t640[8];
#pragma unroll
            for (int t = 1; t < 10; ++t) { t80[t] = twid((float)((lane >> 3) * t) * (1.0f / 80.0f)); PIN(t80[t].x); PIN(t80[t].y); }
#pragma unroll
            for (int a = 1; a < 8; ++a) { t640[a] = twid((float)(r * a) * (1.0f / 640.0f)); PIN(t640[a].x); PIN(t640[a].y); }
            cf twq = twid((float)(s0 * (lane & 7)) * (1.0f / 2048.0f));
            SB();
            STAMP(2, 11);
            char* bufA = smem + L3_A;
            char* bufB = smem + L3_B;
            if (wv < 4) {
                const int ve = vscale_exp(wm);
                const float NORM = (1.0f / (2048.0f * 640.0f)) * __builtin_bit_cast(float, (unsigned)ve << 23);
                cf f[10];
                if (wv == 0) {
                    const cf w4 = {rdlane(twq.x, 4) * NORM, rdlane(twq.y, 4) * NORM};
#pragma unroll
                    for (int bb = 0; bb < 10; ++bb) {
                        cf A = {(float)u[bb].x * NORM, (float)u[bb].y * NORM}, B = cmulc(cf{(float)u2[bb].x, (float)u2[bb].y}, w4);
                        if (bb == 0 || bb == 5) { if (lane == 0) { A.y = 0.f; B.y = 0.f; } }
                        f[bb] = bb < 5 ? cf{A.x - B.y, A.y + B.x} : cf{A.x + B.y, B.x - A.y};
                    }
                } else {
                    const float N2 = 2.0f * NORM;
                    const cf wa = {rdlane(twq.x, wv) * N2, rdlane(twq.y, wv) * N2}, wb = {rdlane(twq.x, 8 - wv) * N2, rdlane(twq.y, 8 - wv) * N2};
#pragma unroll
                    for (int bb = 0; bb < 5; ++bb) f[bb] = cmulc(cf{(float)u[bb].x, (float)u[bb].y}, wa);
#pragma unroll
                    for (int bb = 5; bb < 10; ++bb) { cf t = cmulc(cf{(float)u[bb].x, (float)u[bb].y}, wb); f[bb] = cf{t.x, -t.y}; }
                    const cf g0 = cmulc(cf{(float)e0.x, (float)e0.y}, wb), g1 = cmulc(cf{(float)e1.x, (float)e1.y}, wa);
                    if (lane == 0) {
                        f[0] = cf{0.5f * (f[0].x + g0.x), 0.5f * (f[0].y - g0.y)};
                        f[5] = cf{0.5f * (f[5].x + g1.x), 0.5f * (f[5].y + g1.y)};
                    }
                }
                STAMP(2, 1);
                fft10<1>(f);
                lds_st(bufA, wv * 720 + lane, f[0]);
#pragma unroll
                for (int t = 1; t < 10; ++t) lds_st(bufA, wv * 720 + t * 72 + lane, cmulc(f[t], t80[t]));
            }
            STAMP(2, 2);
            helper_barrier(smem, lane, 5);
            STAMP(2, 13);
            cf a8[8];
            {
                const int base = qr * 720 + (r >> 3) * 72 + (r & 7);
#pragma unroll
                for (int i = 0; i < 8; ++i) a8[i] = lds_ld(bufA, base + 8 * i);
                fft8<1>(a8);
                const int ob = qr * 656 + (r & 7) * 82 + (r >> 3);
#pragma unroll
                for (int i = 0; i < 8; ++i) lds_st(bufB, ob + 10 * i, a8[i]);
            }
            STAMP(2, 14);
            helper_barrier(smem, lane, 10);
            STAMP(2, 15);
            {
                a8[0] = lds_ld(bufB, qr * 656 + r);
#pragma unroll
                for (int a = 1; a < 8; ++a) a8[a] = cmulc(lds_ld(bufB, qr * 656 + a * 82 + r), t640[a]);
                fft8<1>(a8);
#pragma unroll
                for (int i = 0; i < 8; ++i) lds_st(bufA, qr * 720 + r + 80 * i, a8[i]);
            }
            helper_barrier(smem, lane, 15);
            STAMP(2, 3);
            {
                f32x4 p[4];
#pragma unroll
                for (int q = 0; q < 4; ++q) p[q] = *(const LAS f32x4*)((LAS const char*)bufA + (q * 720 + 2 * tid) * 8);
                float y[2][8];
#pragma unroll
                for (int e = 0; e < 2; ++e) {
                    const float P0 = p[0][2 * e], P4 = p[0][2 * e + 1];
                    const float A = P0 + P4, B = P0 - P4;
                    const float c0 = A + p[2][2 * e], c2 = A - p[2][2 * e], c1 = B - p[2][2 * e + 1], c3 = B + p[2][2 * e + 1];
                    const float o0 = p[1][2 * e] + p[3][2 * e], o2 = p[3][2 * e + 1] - p[1][2 * e + 1];
                    const float uu = p[1][2 * e] - p[3][2 * e], vv = p[1][2 * e + 1] + p[3][2 * e + 1];
                    const float o1 = uu - vv, o3 = -uu - vv;
                    y[e][0] = c0 + o0; y[e][4] = c0 - o0;
                    y[e][2] = c2 + o2; y[e][6] = c2 - o2;
                    y[e][1] = c1 + RH * o1; y[e][5] = c1 - RH * o1;
                    y[e][3] = c3 + RH * o3; y[e][7] = c3 - RH * o3;
                }
                char* stg = bufB + wv * 2048;
#pragma unroll
                for (int j = 0; j < 8; ++j) *(LAS unsigned*)((LAS char*)stg + j * 256 + lane * 4) = pack_bf16(y[0][j], y[1][j]);
                wave_fence();
                __builtin_amdgcn_s_setprio(3);
#pragma unroll
                for (int hh = 0; hh < 2; ++hh) {
                    const int j = 4 * hh + (lane >> 4);
                    u32x4 o = *(const LAS u32x4*)((LAS const char*)stg + hh * 1024 + lane * 16);
                    st16_wt(sg + (size_t)(s0 + 256 * j) * (D_ * 2) + wv * 256 + (lane & 15) * 16, o);
                }
            }
            __builtin_amdgcn_s_setprio(0);
            STAMP(2, 4);
        }
        const int r0 = tid / 10, c0 = tid - r0 * 10, p1 = 512 + (tid >> 2), r1 = p1 / 10, c1 = p1 - r1 * 10;
        const size_t eo0 = (size_t)(mt2 * 8 + (r0 >> 3) + 256 * (r0 & 7)) * (D_ * 2) + nt2 * 160 + c0 * 16;
        const size_t eo1 = (size_t)(mt2 * 8 + (r1 >> 3) + 256 * (r1 & 7)) * (D_ * 2) + nt2 * 160 + c1 * 16 + (tid & 3) * 4;
        SB();
        while (lds_min4(smem, L3_CNT + 16) == 0u) __builtin_amdgcn_s_sleep(2);
        __builtin_amdgcn_s_setprio(3);
        STAMP(2, 7);
        const u32x4 xa = *(const u32x4*)(x + eo0);
        const unsigned xb = *(const unsigned*)(x + eo1);
        u32x4 sa; unsigned sb;
        {
            const char* src0 = sg + eo0;
            const char* src1 = sg + eo1;
            for (unsigned spin = 0; spin < (1u << 20); ++spin) {
                asm volatile("global_load_dwordx4 %0, %2, off sc1\n\tglobal_load_dword %1, %3, off sc1\n\ts_waitcnt vmcnt(0)"
                             : "=&v"(sa), "=&v"(sb) : "v"(src0), "v"(src1) : "memory");
                const bool bad = sa.x == CANARY || sa.y == CANARY || sa.z == CANARY || sa.w == CANARY || sb == CANARY;
                if (__builtin_amdgcn_ballot_w64(bad) == 0ull) break;
                __builtin_amdgcn_s_sleep(8);
            }
        }
        STAMP(2, 8);
        bar();
        STAMP(2, 9);
        {
            const char* rp = smem + L3_RED + r0 * RPITCH + c0 * 32;
            const char* rq = smem + L3_RED + r1 * RPITCH + c1 * 32 + (tid & 3) * 8;
            f32x4 o0 = *(const LAS f32x4*)((LAS const char*)rp), o1 = *(const LAS f32x4*)((LAS const char*)rp + 16);
            f32x2 o2 = *(const LAS f32x2*)((LAS const char*)rq);
#pragma unroll
            for (int w = 1; w < 4; ++w) {
                o0 += *(const LAS f32x4*)((LAS const char*)rp + w * (64 * RPITCH));
                o1 += *(const LAS f32x4*)((LAS const char*)rp + w * (64 * RPITCH) + 16);
                o2 += *(const LAS f32x2*)((LAS const char*)rq + w * (64 * RPITCH));
            }
            const float o[8] = {o0.x, o0.y, o0.z, o0.w, o1.x, o1.y, o1.z, o1.w};
            u32x4 res;
#pragma unroll
            for (int i = 0; i < 4; ++i)
                res[i] = pack_bf16(bf_lo(xa[i]) + bf_lo(sa[i]) * silu_f(o[2 * i]), bf_hi(xa[i]) + bf_hi(sa[i]) * silu_f(o[2 * i + 1]));
            *(u32x4*)(out + eo0) = res;
            *(unsigned*)(out + eo1) = pack_bf16(bf_lo(xb) + bf_lo(sb) * silu_f(o2.x), bf_hi(xb) + bf_hi(sb) * silu_f(o2.y));
        }
        STAMP(2, 10); STAMP_FLUSH(2);
    }
}

extern "C" void kernel_launch(void* const* d_in, const int* in_sizes, int n_in, void* d_out, int out_size, void* d_ws,
                              size_t ws_size, hipStream_t stream) {
    (void)in_sizes; (void)n_in; (void)out_size; (void)ws_size;
    char* ws = (char*)d_ws;
    (void)hipFuncSetAttribute((const void*)k12_kernel, hipFuncAttributeMaxDynamicSharedMemorySize, K2_LDS);
    (void)hipFuncSetAttribute((const void*)k3_kernel, hipFuncAttributeMaxDynamicSharedMemorySize, K3_LDS);
    k12_kernel<<<256, 512, K2_LDS, stream>>>((const unsigned*)d_in[0], (const char*)d_in[4], (const char*)d_in[5], (const char*)d_in[6],
                                             (const unsigned short*)d_in[1], (const unsigned short*)d_in[2], ws);
    k3_kernel<<<256, 768, K3_LDS, stream>>>((const char*)d_in[0], ws, (char*)d_out);
}
```

```cpp
#include <hip/hip_runtime.h>
#include <hip/hip_bf16.h>
#include <stdint.h>

typedef float f32x2 __attribute__((ext_vector_type(2)));
typedef float f32x4 __attribute__((ext_vector_type(4)));
typedef float f32x16 __attribute__((ext_vector_type(16)));
typedef short s16x4 __attribute__((ext_vector_type(4)));
typedef __bf16 bf16x2 __attribute__((ext_vector_type(2)));
typedef __bf16 bf16x8 __attribute__((ext_vector_type(8)));
typedef _Float16 f16x2 __attribute__((ext_vector_type(2)));
typedef unsigned u32x2 __attribute__((ext_vector_type(2)));
typedef unsigned u32x4 __attribute__((ext_vector_type(4)));

#define DEV __device__ __forceinline__
#define LAS __attribute__((address_space(3)))
#define GAS __attribute__((address_space(1)))
#if defined(__HIP_DEVICE_COMPILE__)
#define NOPK __attribute__((target("no-packed-fp32-ops")))
#else
#define NOPK
#endif

constexpr int D_ = 640, F_ = 1536, DF_ = 321;
constexpr int NG4 = 81;

constexpr size_t WS_XN = 0;
constexpr size_t WS_T = 4u << 20;
constexpr size_t WS_WT = 12u << 20;
constexpr size_t WS_W2T = 16u << 20;
constexpr size_t WS_HT = 20u << 20;
constexpr size_t WS_S = 28u << 20;
constexpr size_t WS_T16 = 32u << 20;
constexpr size_t WS_SYNC = 37u << 20;
constexpr size_t WS_WMAX = 36u << 20;
constexpr int TP = 328;

struct cf { float x, y; };
DEV cf operator+(cf a, cf b) { return {a.x + b.x, a.y + b.y}; }
DEV cf operator-(cf a, cf b) { return {a.x - b.x, a.y - b.y}; }
DEV cf cmul(cf a, cf w) { return {a.x * w.x - a.y * w.y, a.x * w.y + a.y * w.x}; }
DEV cf cmulc(cf a, cf w) { return {a.x * w.x + a.y * w.y, a.y * w.x - a.x * w.y}; }
template <int DIR> DEV cf muli(cf a) { return DIR > 0 ? cf{-a.y, a.x} : cf{a.y, -a.x}; }
DEV cf twid(float f) { return {__builtin_amdgcn_cosf(f), -__builtin_amdgcn_sinf(f)}; }

DEV unsigned pack_bf16(float a, float b) {
    f32x2 v = {a, b};
    return __builtin_bit_cast(unsigned, __builtin_convertvector(v, bf16x2));
}
DEV float bf_lo(unsigned u) { return __uint_as_float(u << 16); }
DEV float bf_hi(unsigned u) { return __uint_as_float(u & 0xffff0000u); }
DEV float rbf(float a) { return bf_lo(pack_bf16(a, a)); }

template <int DIR> DEV void fft4(cf& a0, cf& a1, cf& a2, cf& a3) {
    cf t0 = a0 + a2, t1 = a0 - a2, t2 = a1 + a3, t3 = muli<DIR>(a1 - a3);
    a0 = t0 + t2; a2 = t0 - t2; a1 = t1 + t3; a3 = t1 - t3;
}
constexpr float RH = 0.70710678118654752f;
template <int DIR> DEV cf mulw8(cf a) { return DIR > 0 ? cf{(a.x - a.y) * RH, (a.x + a.y) * RH} : cf{(a.x + a.y) * RH, (a.y - a.x) * RH}; }
template <int DIR> DEV cf mulw83(cf a) { return DIR > 0 ? cf{-(a.x + a.y) * RH, (a.x - a.y) * RH} : cf{(a.y - a.x) * RH, -(a.x + a.y) * RH}; }

template <int DIR> DEV void fft8(cf (&a)[8]) {
    fft4<DIR>(a[0], a[2], a[4], a[6]);
    fft4<DIR>(a[1], a[3], a[5], a[7]);
    cf o1 = mulw8<DIR>(a[3]), o2 = muli<DIR>(a[5]), o3 = mulw83<DIR>(a[7]);
    cf e0 = a[0], e1 = a[2], e2 = a[4], e3 = a[6], o0 = a[1];
    a[0] = e0 + o0; a[4] = e0 - o0;
    a[1] = e1 + o1; a[5] = e1 - o1;
    a[2] = e2 + o2; a[6] = e2 - o2;
    a[3] = e3 + o3; a[7] = e3 - o3;
}
template <int DIR> DEV void fft5(cf (&a)[5]) {
    constexpr float c1 = 0.30901699437494742f, c2 = -0.80901699437494742f;
    constexpr float s1 = 0.95105651629515357f, s2 = 0.58778525229247313f;
    cf t1 = a[1] + a[4], t2 = a[2] + a[3], t3 = a[1] - a[4], t4 = a[2] - a[3];
    cf m1 = {a[0].x + c1 * t1.x + c2 * t2.x, a[0].y + c1 * t1.y + c2 * t2.y};
    cf m2 = {a[0].x + c2 * t1.x + c1 * t2.x, a[0].y + c2 * t1.y + c1 * t2.y};
    cf n1 = muli<DIR>(cf{s1 * t3.x + s2 * t4.x, s1 * t3.y + s2 * t4.y});
    cf n2 = muli<DIR>(cf{s2 * t3.x - s1 * t4.x, s2 * t3.y - s1 * t4.y});
    a[0] = a[0] + t1 + t2;
    a[1] = m1 + n1; a[4] = m1 - n1;
    a[2] = m2 + n2; a[3] = m2 - n2;
}
template <int DIR> DEV void fft10(cf (&a)[10]) {
    cf e[5], o[5];
#pragma unroll
    for (int n2 = 0; n2 < 5; ++n2) { const cf p = a[(2 * n2) % 10], q = a[(2 * n2 + 5) % 10]; e[n2] = p + q; o[n2] = p - q; }
    fft5<DIR>(e); fft5<DIR>(o);
#pragma unroll
    for (int k2 = 0; k2 < 5; ++k2) { a[(6 * k2) % 10] = e[k2]; a[(6 * k2 + 5) % 10] = o[k2]; }
}
template <int DIR> DEV void fft16_a(cf (&a)[16]) {
#pragma unroll
    for (int n2 = 0; n2 < 4; ++n2) fft4<DIR>(a[n2], a[n2 + 4], a[n2 + 8], a[n2 + 12]);
}
template <int DIR> DEV void fft16_t(cf (&a)[16]) {
    constexpr float C1 = 0.92387953251128674f, S1 = 0.38268343236508977f;
    const cf w1 = {C1, DIR * S1}, w3 = {S1, DIR * C1}, w9 = {-C1, -DIR * S1};
    a[1 + 4] = cmul(a[1 + 4], w1);
    a[1 + 8] = mulw8<DIR>(a[1 + 8]);
    a[1 + 12] = cmul(a[1 + 12], w3);
    a[2 + 4] = mulw8<DIR>(a[2 + 4]);
    a[2 + 8] = muli<DIR>(a[2 + 8]);
    a[2 + 12] = mulw83<DIR>(a[2 + 12]);
    a[3 + 4] = cmul(a[3 + 4], w3);
    a[3 + 8] = mulw83<DIR>(a[3 + 8]);
    a[3 + 12] = cmul(a[3 + 12], w9);
}
template <int DIR> DEV void fft16_b(cf (&a)[16]) {
#pragma unroll
    for (int k1 = 0; k1 < 4; ++k1) fft4<DIR>(a[4 * k1], a[4 * k1 + 1], a[4 * k1 + 2], a[4 * k1 + 3]);
    cf b[16];
#pragma unroll
    for (int k1 = 0; k1 < 4; ++k1)
#pragma unroll
        for (int k2 = 0; k2 < 4; ++k2) b[k1 + 4 * k2] = a[4 * k1 + k2];
#pragma unroll
    for (int i = 0; i < 16; ++i) a[i] = b[i];
}

template <int CTRL, int RM> DEV float dpp_add(float v) {
    int t = __builtin_amdgcn_update_dpp(0, __float_as_int(v), CTRL, RM, 0xf, true);
    return v + __int_as_float(t);
}
DEV float wave_sum(float v) {
    v = dpp_add<0x111, 0xf>(v);
    v = dpp_add<0x112, 0xf>(v);
    v = dpp_add<0x114, 0xf>(v);
    v = dpp_add<0x118, 0xf>(v);
    v = dpp_add<0x142, 0xa>(v);
    v = dpp_add<0x143, 0xc>(v);
    return __int_as_float(__builtin_amdgcn_readlane(__float_as_int(v), 63));
}
template <int CTRL, int RM> DEV unsigned dpp_max(unsigned v) {
    const unsigned t = (unsigned)__builtin_amdgcn_update_dpp(0, (int)v, CTRL, RM, 0xf, true);
    return v > t ? v : t;
}
DEV unsigned wave_max(unsigned v) {
    v = dpp_max<0x111, 0xf>(v); v = dpp_max<0x112, 0xf>(v); v = dpp_max<0x114, 0xf>(v); v = dpp_max<0x118, 0xf>(v);
    v = dpp_max<0x142, 0xa>(v); v = dpp_max<0x143, 0xc>(v);
    return (unsigned)__builtin_amdgcn_readlane((int)v, 63);
}
struct WMax { u32x4 a, b, c; };
DEV WMax vscale_load(const char* ws, int lane) { const u32x4* t = (const u32x4*)(ws + WS_WMAX) + lane; return {t[0], t[64], t[128]}; }
DEV int vscale_exp(const WMax& w) {
    const u32x4 a = w.a, b = w.b, c = w.c;
    unsigned m = a.x;
#pragma unroll
    for (int i = 0; i < 4; ++i) { m = m > a[i] ? m : a[i]; m = m > b[i] ? m : b[i]; m = m > c[i] ? m : c[i]; }
    int e = (int)(wave_max(m) >> 7) + 11;
    e = e < 16 ? 16 : e;
    return e > 238 ? 238 : e;
}
DEV float rdlane(float v, int l) { return __int_as_float(__builtin_amdgcn_readlane(__float_as_int(v), l)); }
#define PIN(x) asm volatile("" : "+v"(x))
#define SB() __builtin_amdgcn_sched_barrier(0)
DEV void bar() { SB(); __builtin_amdgcn_s_barrier(); asm volatile("" ::: "memory"); SB(); }
DEV void wait_lgkm0() { asm volatile("s_waitcnt lgkmcnt(0)" ::: "memory"); }
DEV void st16_wt(void* p, u32x4 v) { asm volatile("global_store_dwordx4 %0, %1, off sc1\n\ts_nop 1" ::"v"(p), "v"(v) : "memory"); }
DEV void st4_wt(void* p, unsigned v) { asm volatile("global_store_dword %0, %1, off sc1" ::"v"(p), "v"(v) : "memory"); }
DEV int s0_of(int b) { return (b & 3) * 64 + ((b >> 2) & 1) * 32 + (b >> 3); }
#define CODE_ADDR(p, sym) do { const char* pc_; int off_; asm volatile("s_getpc_b64 %0\n\ts_mov_b32 %1, " #sym "@rel32@lo+4" : "=s"(pc_), "=s"(off_)); p = pc_ + off_; } while (0)
DEV void wave_fence() { asm volatile("" ::: "memory"); __builtin_amdgcn_wave_barrier(); }

DEV cf lds_ld(const char* base, int idx) { f32x2 v = *(const LAS f32x2*)((LAS const char*)base + idx * 8); return {v.x, v.y}; }
DEV void lds_st(char* base, int idx, cf a) { f32x2 v = {a.x, a.y}; *(LAS f32x2*)((LAS char*)base + idx * 8) = v; }

typedef f32x2 c2;
DEV c2 addi(c2 a, c2 b) { c2 r; asm("v_pk_add_f32 %0, %1, %2 op_sel:[0,1] op_sel_hi:[1,0] neg_lo:[0,1]" : "=v"(r) : "v"(a), "v"(b)); return r; }
DEV c2 subi(c2 a, c2 b) { c2 r; asm("v_pk_add_f32 %0, %1, %2 op_sel:[0,1] op_sel_hi:[1,0] neg_hi:[0,1]" : "=v"(r) : "v"(a), "v"(b)); return r; }
DEV c2 addc(c2 a, c2 b) { c2 r; asm("v_pk_add_f32 %0, %1, %2 neg_hi:[0,1]" : "=v"(r) : "v"(a), "v"(b)); return r; }
DEV c2 subc(c2 a, c2 b) { c2 r; asm("v_pk_add_f32 %0, %1, %2 neg_lo:[0,1]" : "=v"(r) : "v"(a), "v"(b)); return r; }
DEV c2 pmul(c2 a, c2 w) {
    c2 t = a.xx * w, r;
    asm("v_pk_fma_f32 %0, %1, %2, %3 op_sel:[1,1,0] op_sel_hi:[1,0,1] neg_lo:[0,1,0]" : "=v"(r) : "v"(a), "v"(w), "v"(t));
    return r;
}
DEV c2 pmulc(c2 a, c2 w) {
    c2 t = a.xx * w, r;
    asm("v_pk_fma_f32 %0, %1, %2, %3 op_sel:[1,1,0] op_sel_hi:[1,0,1] neg_hi:[0,1,0]" : "=v"(r) : "v"(a), "v"(w), "v"(t));
    return r;
}
DEV c2 pfma(c2 a, float k, c2 c) { return __builtin_elementwise_fma(a, c2{k, k}, c); }
DEV c2 ptw(float f) { c2 r = {__builtin_amdgcn_cosf(f), -__builtin_amdgcn_sinf(f)}; PIN(r); return r; }
DEV void pfft4(c2& a0, c2& a1, c2& a2, c2& a3) {
    c2 t0 = a0 + a2, t1 = a0 - a2, t2 = a1 + a3, t3 = a1 - a3;
    a0 = t0 + t2; a2 = t0 - t2; a1 = subi(t1, t3); a3 = addi(t1, t3);
}
DEV void pfft8(c2 (&a)[8]) {
    pfft4(a[0], a[2], a[4], a[6]);
    pfft4(a[1], a[3], a[5], a[7]);
    const c2 e0 = a[0], e1 = a[2], e2 = a[4], e3 = a[6], o0 = a[1], o2 = a[5];
    const c2 t1 = subi(a[3], a[3]), t3 = addi(a[7], a[7]);
    a[0] = e0 + o0; a[4] = e0 - o0;
    a[1] = pfma(t1, RH, e1); a[5] = pfma(t1, -RH, e1);
    a[2] = subi(e2, o2); a[6] = addi(e2, o2);
    a[3] = pfma(t3, -RH, e3); a[7] = pfma(t3, RH, e3);
}
DEV void pfft5(c2 (&a)[5]) {
    constexpr float c1 = 0.30901699437494742f, cc2 = -0.80901699437494742f;
    constexpr float s1 = 0.95105651629515357f, s2 = 0.58778525229247313f;
    const c2 t1 = a[1] + a[4], t2 = a[2] + a[3], t3 = a[1] - a[4], t4 = a[2] - a[3];
    const c2 m1 = pfma(t2, cc2, pfma(t1, c1, a[0])), m2 = pfma(t2, c1, pfma(t1, cc2, a[0]));
    const c2 n1 = pfma(t4, s2, t3 * s1), n2 = pfma(t4, -s1, t3 * s2);
    a[0] = a[0] + t1 + t2;
    a[1] = subi(m1, n1); a[4] = addi(m1, n1);
    a[2] = subi(m2, n2); a[3] = addi(m2, n2);
}
DEV void pfft10(c2 (&a)[10]) {
    c2 e[5], o[5];
#pragma unroll
    for (int n2 = 0; n2 < 5; ++n2) { const c2 p = a[(2 * n2) % 10], q = a[(2 * n2 + 5) % 10]; e[n2] = p + q; o[n2] = p - q; }
    pfft5(e); pfft5(o);
#pragma unroll
    for (int k2 = 0; k2 < 5; ++k2) { a[(6 * k2) % 10] = e[k2]; a[(6 * k2 + 5) % 10] = o[k2]; }
}
DEV c2 pld(const char* base, int idx) { return *(const LAS c2*)((LAS const char*)base + idx * 8); }
DEV void pst(char* base, int idx, c2 a) { *(LAS c2*)((LAS char*)base + idx * 8) = a; }
DEV unsigned pack_f16(c2 a) { return __builtin_bit_cast(unsigned, __builtin_convertvector(a, f16x2)); }

constexpr unsigned CANARY = 0x7f817f81u;
constexpr int K3_CODE = 10240;
constexpr int BUFA = 4 * 720 * 8, XP = 1296;
constexpr int WGRP = 3104, SLOT = 4 * WGRP, NS = 10, KSTEPS = 20;
constexpr int FSCR = 16 * 68 * 8;
#define X_WAIT 20
#define P1_WAIT 36
constexpr int L2_F = NS * SLOT, L2_FLAG = L2_F + 3 * FSCR;
constexpr int L2_CNT = L2_FLAG + 32, L2_PUB = L2_FLAG + 36, L2_GO = L2_FLAG + 40, L2_TAG = L2_FLAG + 44;
constexpr int L2_P1 = 6 * SLOT;
constexpr int L2_W2 = L2_FLAG + 64;
constexpr int K2_LDS = L2_W2 + 10240;

template <int N> DEV void wait_vm() { asm volatile("s_waitcnt vmcnt(%0)" ::"i"(N) : "memory"); }
DEV void touch(const char* p, unsigned lds) { asm volatile("s_mov_b32 m0, %1\n\ts_nop 0\n\tglobal_load_lds_dword %0, off" :: "v"(p), "s"(lds) : "memory"); }
DEV void sw_barrier(char* smem, int off, int lane, unsigned target) {
    wait_lgkm0();
    LAS unsigned* cnt = (LAS unsigned*)((LAS char*)smem + off);
    if (lane == 0) __hip_atomic_fetch_add(cnt, 1u, __ATOMIC_RELAXED, __HIP_MEMORY_SCOPE_WORKGROUP);
    while (__hip_atomic_load(cnt, __ATOMIC_RELAXED, __HIP_MEMORY_SCOPE_WORKGROUP) < target) {}
    asm volatile("" ::: "memory");
}
DEV void lds_inc(char* smem, int off, int lane) {
    if (lane == 0) __hip_atomic_fetch_add((LAS unsigned*)((LAS char*)smem + off), 1u, __ATOMIC_RELAXED, __HIP_MEMORY_SCOPE_WORKGROUP);
}
DEV void ln_row(const unsigned (&z)[5], char* dst, int lane) {
        float v[10];
#pragma unroll
        for (int i = 0; i < 5; ++i) { v[2 * i] = bf_lo(z[i]); v[2 * i + 1] = bf_hi(z[i]); }
        const float c0 = rdlane(v[0], 0);
        float s1 = 0.f, s2 = 0.f;
#pragma unroll
        for (int i = 0; i < 10; ++i) { float d = v[i] - c0; s1 += d; s2 += d * d; }
        s1 = wave_sum(s1); s2 = wave_sum(s2);
        const float md = s1 * (1.0f / 640.0f);
        float var = s2 * (1.0f / 640.0f) - md * md;
        var = var < 0.f ? 0.f : var;
        const float m = rbf(c0 + md);
        const float sd = rbf(__builtin_amdgcn_sqrtf(rbf(rbf(var) + 1e-5f)));
        const float rs = __builtin_amdgcn_rcpf(sd);
#pragma unroll
        for (int i = 0; i < 5; ++i) {
            unsigned d = pack_bf16(v[2 * i] - m, v[2 * i + 1] - m);
            *(LAS unsigned*)((LAS char*)dst + (lane + 64 * i) * 4) = pack_bf16(bf_lo(d) * rs, bf_hi(d) * rs);
        }
}
static __device__ __attribute__((noinline)) void row_phase(const GAS unsigned* x32_, const GAS unsigned short* wr, const GAS unsigned short* wi, GAS char* ws_, unsigned lds_) {
    char* smem = (char*)(LAS char*)(uintptr_t)__builtin_amdgcn_readfirstlane(lds_);
    const int b = __builtin_amdgcn_workgroup_id_x(), tid = __builtin_amdgcn_workitem_id_x(), lane = tid & 63;
    const int wv = __builtin_amdgcn_readfirstlane(tid >> 6);
    char* ws = (char*)ws_;
    {
        const int s0 = s0_of(b);
        const int s = s0 + 256 * wv;
        unsigned z[5];
        const GAS unsigned* xp = x32_ + s * 320 + lane;
        asm volatile("global_load_dword %0, %5, off\n\tglobal_load_dword %1, %5, off offset:256\n\tglobal_load_dword %2, %5, off offset:512\n\t"
                     "global_load_dword %3, %5, off offset:768\n\tglobal_load_dword %4, %5, off offset:1024"
                     : "=&v"(z[0]), "=&v"(z[1]), "=&v"(z[2]), "=&v"(z[3]), "=&v"(z[4]) : "v"(xp) : "memory");
        const bool wtask = wv >= 1 && wv <= 3;
        const int task = b * 3 + wv - 1, id = task / 6, wq_ = id & 7, wa_ = id >> 3, k = (task - id * 6) * 64 + lane, kc = k < DF_ ? k : DF_ - 1;
        unsigned re[16], im[16];
        if (wtask) {
            const GAS unsigned short* pr = wr + (8 * wa_ + wq_) * DF_ + kc;
            const GAS unsigned short* pi = wi + (8 * wa_ + wq_) * DF_ + kc;
#pragma unroll
            for (int i = 0; i < 16; ++i)
                asm volatile("global_load_ushort %0, %2, off\n\tglobal_load_ushort %1, %3, off" : "=&v"(re[i]), "=&v"(im[i]) : "v"(pr + i * (128 * DF_)), "v"(pi + i * (128 * DF_)) : "memory");
        }
        const int qr = tid / 80, r = tid - qr * 80;
        c2 t640[8], t80[8];
#pragma unroll
        for (int a = 1; a < 8; ++a) t640[a] = ptw((float)(r * a) * (1.0f / 640.0f));
#pragma unroll
        for (int a = 1; a < 8; ++a) t80[a] = ptw((float)((r >> 3) * a) * (1.0f / 80.0f));
        const c2 twq = ptw((float)(s0 * (lane & 7)) * (1.0f / 2048.0f));
        SB();
        if (wtask) wait_vm<32>(); else wait_vm<0>();
        asm volatile("" : "+v"(z[0]), "+v"(z[1]), "+v"(z[2]), "+v"(z[3]), "+v"(z[4]));
        char* bufA = smem + L2_P1;
        char* bufB = bufA + BUFA;
        ln_row(z, bufB + wv * XP, lane);
        wait_lgkm0();
        bar();
        {
            c2 xr[8];
#pragma unroll
            for (int j = 0; j < 8; ++j) { const unsigned w = *(const LAS unsigned*)((LAS const char*)bufB + j * XP + tid * 4); xr[j] = c2{bf_lo(w), bf_hi(w)}; }
            const c2 a0 = xr[0] + xr[4], a1 = xr[0] - xr[4], b0 = xr[2] + xr[6], b1 = xr[2] - xr[6];
            const c2 e0 = xr[1] + xr[5], e1 = xr[1] - xr[5], d0 = xr[3] + xr[7], d1 = xr[3] - xr[7];
            const c2 p = a0 + b0, q = e0 + d0, u = e1 - d1, w = e1 + d1;
            const c2 y0 = p + q, y4 = p - q, y2r = a0 - b0, y2i = d0 - e0;
            const c2 y1r = pfma(u, RH, a1), y1i = pfma(w, -RH, -b1), y3r = pfma(u, -RH, a1), y3i = pfma(w, -RH, b1);
            *(LAS f32x4*)((LAS char*)bufA + (0 * 720 + 2 * tid) * 8) = f32x4{y0.x, y4.x, y0.y, y4.y};
            *(LAS f32x4*)((LAS char*)bufA + (1 * 720 + 2 * tid) * 8) = f32x4{y1r.x, y1i.x, y1r.y, y1i.y};
            *(LAS f32x4*)((LAS char*)bufA + (2 * 720 + 2 * tid) * 8) = f32x4{y2r.x, y2i.x, y2r.y, y2i.y};
            *(LAS f32x4*)((LAS char*)bufA + (3 * 720 + 2 * tid) * 8) = f32x4{y3r.x, y3i.x, y3r.y, y3i.y};
        }
        wait_lgkm0();
        bar();
        c2 a8[8];
        {
#pragma unroll
            for (int i = 0; i < 8; ++i) a8[i] = pld(bufA, qr * 720 + r + 80 * i);
            pfft8(a8);
            pst(bufB, qr * 656 + r, a8[0]);
#pragma unroll
            for (int a = 1; a < 8; ++a) pst(bufB, qr * 656 + a * 82 + r, pmul(a8[a], t640[a]));
        }
        sw_barrier(smem, L2_CNT, lane, 5);
        {
            const int ib = qr * 656 + (r & 7) * 82 + (r >> 3), ob = qr * 720 + (r >> 3) * 72 + (r & 7);
#pragma unroll
            for (int i = 0; i < 8; ++i) a8[i] = pld(bufB, ib + 10 * i);
            pfft8(a8);
            pst(bufA, ob, a8[0]);
#pragma unroll
            for (int a = 1; a < 8; ++a) pst(bufA, ob + 8 * a, pmul(a8[a], t80[a]));
        }
        sw_barrier(smem, L2_CNT, lane, 10);
        if (wv < 4) {
            c2 f[10];
#pragma unroll
            for (int t = 0; t < 10; ++t) f[t] = pld(bufA, wv * 720 + t * 72 + lane);
            pfft10(f);
            unsigned* T = (unsigned*)(ws + WS_T16) + (size_t)s0 * (8 * TP);
            if (wv == 0) {
#pragma unroll
                for (int bb = 5; bb < 10; ++bb) pst(bufB, lane + 64 * (bb - 5), f[bb]);
                if (lane == 0) pst(bufB, 320, f[0]);
                wave_fence();
                const c2 w4 = {rdlane(twq.y, 4) * 0.5f, rdlane(twq.x, 4) * -0.5f};
#pragma unroll
                for (int bb = 0; bb < 5; ++bb) {
                    const c2 h = pld(bufB, 320 - lane - 64 * bb);
                    st4_wt(T + (lane + 64 * bb), pack_f16(addc(f[bb], h) * 0.5f));
                    st4_wt(T + (4 * TP + lane + 64 * bb), pack_f16(pmul(subc(f[bb], h), w4)));
                }
                if (lane == 0) {
                    st4_wt(T + (320), pack_f16(c2{f[5].x, 0.f}));
                    st4_wt(T + (4 * TP + 320), pack_f16(pmul(subc(f[5], f[5]), w4)));
                }
            } else {
                const c2 wa = {rdlane(twq.x, wv), rdlane(twq.y, wv)}, wb = {rdlane(twq.x, 8 - wv), rdlane(twq.y, 8 - wv)};
                unsigned* Ta = T + wv * TP;
                unsigned* Tb = T + (8 - wv) * TP;
#pragma unroll
                for (int bb = 0; bb < 5; ++bb) st4_wt(Ta + (lane + 64 * bb), pack_f16(pmul(f[bb], wa)));
#pragma unroll
                for (int bb = 5; bb < 10; ++bb) st4_wt(Tb + (640 - lane - 64 * bb), pack_f16(pmulc(f[bb], wb)));
                if (lane == 0) {
                    st4_wt(Tb + (0), pack_f16(pmulc(f[0], wb)));
                    st4_wt(Ta + (320), pack_f16(pmul(f[5], wa)));
                }
                wait_vm<12>();
                unsigned o[16], mx = 0u;
#pragma unroll
                for (int i = 0; i < 16; ++i) {
                    asm volatile("" : "+v"(re[i]), "+v"(im[i]));
                    o[i] = re[i] | (im[i] << 16);
                    const unsigned ma = re[i] & 0x7fffu, mb = im[i] & 0x7fffu;
                    mx = mx > ma ? mx : ma; mx = mx > mb ? mx : mb;
                }
                mx = wave_max(mx);
                if (lane == 0) st4_wt((unsigned*)(ws + WS_WMAX) + task, mx);
                if (k < 4 * NG4) {
                    u32x4* Wt = (u32x4*)(ws + WS_WT) + (wq_ * NG4 + (k >> 2)) * 256 + wa_ * 4 + (k & 3);
#pragma unroll
                    for (int bq = 0; bq < 4; ++bq) st16_wt(Wt + bq * 64, u32x4{o[4 * bq], o[4 * bq + 1], o[4 * bq + 2], o[4 * bq + 3]});
                }
            }
        }

    }
}

DEV unsigned lds_min4(const char* smem, int off) {
    u32x4 c = *(const volatile LAS u32x4*)((LAS const char*)smem + off);
    unsigned a = c.x < c.y ? c.x : c.y, b = c.z < c.w ? c.z : c.w;
    return __builtin_amdgcn_readfirstlane(a < b ? a : b);
}
DEV unsigned lds_word(const char* smem, int off) { return __builtin_amdgcn_readfirstlane(*(const volatile LAS unsigned*)((LAS const char*)smem + off)); }
struct Dma { unsigned wo[3]; const char* w1b; const char* w3b; unsigned lw; char* smem; char* ws; };
constexpr int EARLY = 6;
constexpr int issued_before(int p) { int n = p < EARLY ? EARLY : p + 3; return n > KSTEPS ? KSTEPS : n; }
template <int ST> DEV void dma_stage(const Dma& d) {
    if constexpr (ST >= NS) {
        while (lds_min4(d.smem, L2_FLAG + 16) < (unsigned)(ST - NS + 1)) __builtin_amdgcn_s_sleep(1);
    }
#pragma unroll
    for (int ks = 0; ks < 2; ++ks) {
        const char* g1 = d.w1b + (size_t)(2 * ST + ks) * (16 * F_ * 2);
        const char* g3 = d.w3b + (size_t)(2 * ST + ks) * (16 * F_ * 2);
        const unsigned l0 = d.lw + (ST % NS) * SLOT + ks * (2 * WGRP), l1 = l0 + WGRP;
        asm volatile("s_mov_b32 m0, %5\n\ts_nop 0\n\t"
                     "global_load_lds_dwordx4 %0, %3\n\t"
                     "global_load_lds_dwordx4 %1, %3 offset:1024\n\t"
                     "global_load_lds_dwordx4 %2, %3 offset:2048\n\t"
                     "s_mov_b32 m0, %6\n\ts_nop 0\n\t"
                     "global_load_lds_dwordx4 %0, %4\n\t"
                     "global_load_lds_dwordx4 %1, %4 offset:1024\n\t"
                     "global_load_lds_dwordx4 %2, %4 offset:2048"
                     :: "v"(d.wo[0]), "v"(d.wo[1]), "v"(d.wo[2]), "s"(g1), "s"(g3), "s"(l0), "s"(l1) : "memory");
    }
}
template <int A, int B> DEV void dma_range(const Dma& d) { if constexpr (A < B) { dma_stage<A>(d); dma_range<A + 1, B>(d); } }
template <int P> DEV void dma_run(const Dma& d) {
    if constexpr (P < KSTEPS) {
        if constexpr (P > 0) dma_range<issued_before(P - 1), issued_before(P)>(d);
        wait_vm<12 * (issued_before(P) - 1 - P)>();
        *(volatile LAS unsigned*)((LAS char*)d.smem + L2_FLAG) = (unsigned)(P + 1);
        dma_run<P + 1>(d);
    }
}

DEV float silu_f(float v) { return v * __builtin_amdgcn_rcpf(1.0f + __builtin_amdgcn_exp2f(v * -1.44269504088896341f)); }
DEV u32x4 wfrag(const char* smem, unsigned a) {
    s16x4 lo = __builtin_amdgcn_ds_read_tr16_b64_v4i16((LAS s16x4*)((LAS char*)smem + a));
    s16x4 hi = __builtin_amdgcn_ds_read_tr16_b64_v4i16((LAS s16x4*)((LAS char*)smem + a + 768));
    return __builtin_bit_cast(u32x4, __builtin_shufflevector(lo, hi, 0, 1, 2, 3, 4, 5, 6, 7));
}

extern "C" __global__ NOPK __launch_bounds__(512, 2) void k12_kernel(const unsigned* __restrict__ x32, const char* __restrict__ w1, const char* __restrict__ w3,
                                                        const char* __restrict__ w2, const unsigned short* __restrict__ wr,
                                                        const unsigned short* __restrict__ wi, char* __restrict__ ws) {
    extern __shared__ __attribute__((aligned(16))) char smem[];
    const int b = blockIdx.x, tid = threadIdx.x, lane = tid & 63;
    const int wv = __builtin_amdgcn_readfirstlane(tid >> 6);
    const int xcd = b & 7, idx = b >> 3;
    const int mt = (xcd & 3) * 4 + (idx & 3), nt = (xcd >> 2) * 8 + (idx >> 2);

    Dma d;
#pragma unroll
    for (int r = 0; r < 3; ++r) {
        const int L = r * 64 + lane, row = L / 12, c = L - row * 12;
        d.wo[r] = (unsigned)(row * (F_ * 2) + c * 16 - r * 1024);
    }
    d.smem = smem; d.ws = ws;
    d.w1b = w1 + nt * 192; d.w3b = w3 + nt * 192;
    d.lw = (unsigned)(uintptr_t)(LAS char*)smem;
    unsigned* sync = (unsigned*)(ws + WS_SYNC);
    if (wv < 5) {
        unsigned lb = d.lw;
        PIN(lb);
        row_phase((const GAS unsigned*)x32, (const GAS unsigned short*)wr, (const GAS unsigned short*)wi, (GAS char*)ws, lb);
    } else {
        const int s0 = s0_of(b);
        const unsigned* xp = x32 + (s0 + 256 * wv) * 320 + lane;
        unsigned z[5], nonce;
        asm volatile("global_load_dword %0, %6, off\n\tglobal_load_dword %1, %6, off offset:256\n\tglobal_load_dword %2, %6, off offset:512\n\t"
                     "global_load_dword %3, %6, off offset:768\n\tglobal_load_dword %4, %6, off offset:1024\n\tglobal_load_dword %5, %7, off sc1"
                     : "=&v"(z[0]), "=&v"(z[1]), "=&v"(z[2]), "=&v"(z[3]), "=&v"(z[4]), "=&v"(nonce) : "v"(xp), "v"(sync) : "memory");
        if (wv == 5) dma_range<0, 1>(d); else if (wv == 6) dma_range<1, 2>(d); else dma_range<2, 3>(d);
        asm volatile("s_waitcnt vmcnt(12)" : "+v"(z[0]), "+v"(z[1]), "+v"(z[2]), "+v"(z[3]), "+v"(z[4]), "+v"(nonce) :: "memory");
        char* bufB = smem + L2_P1 + BUFA;
        if (wv == 6 && lane < 12) *(volatile LAS unsigned*)((LAS char*)smem + L2_FLAG + lane * 4) = lane == 0 ? (unsigned)EARLY : lane == 11 ? nonce + 1u : 0u;
        ln_row(z, bufB + wv * XP, lane);
        wait_lgkm0();
        bar();

        if (wv < 7) {
            u32x4 pc[5];
#pragma unroll
            for (int i = 0; i < 5; ++i) { const int p = tid - 320 + 128 * i; pc[i] = *(const LAS u32x4*)((LAS const char*)bufB + (p & 7) * XP + (p >> 3) * 16); }
            const unsigned tag = lds_word(smem, L2_TAG);
            wait_lgkm0();
            char* xt = ws + WS_XN + (size_t)(s0 >> 4) * (20 * 8192) + ((s0 >> 2) & 3) * 1024 + (s0 & 3) * 128;
#pragma unroll
            for (int i = 0; i < 5; ++i) {
                const int p = tid - 320 + 128 * i, c = p >> 3;
                st16_wt(xt + (c >> 1) * 4096 + (c & 1) * 512 + (p & 7) * 16, pc[i]);
            }
            bar();
            wait_vm<0>();
            if (lane == 0) st4_wt(sync + 1024 + s0 * 4 + (wv - 5), tag);
        } else {
            bar();
            dma_range<3, 6>(d);
        }
        {
            constexpr int R0 = 32 * EARLY, NA = (D_ - R0) * 24, NT = 2 * NA + 15360, PER = NT / 768;
            static_assert(NT % 768 == 0 && PER <= 64, "");
            const int L = (b * 3 + wv - 5) * PER + (lane < PER ? lane : 0);
            const char* p = L < NA ? w1 + (size_t)(R0 * 24 + L) * 128 : L < 2 * NA ? w3 + (size_t)(R0 * 24 + L - NA) * 128 : w2 + (size_t)(L - 2 * NA) * 128;
            const char* p3; CODE_ADDR(p3, k3_kernel);
            const int Lc = (idx & 7) * 48 + (wv - 5) * 16 + lane - PER;
            if (lane >= PER && Lc < K3_CODE / 128) p = p3 + Lc * 128;
            touch(p, d.lw + L2_W2);
        }

    }

    if (wv == 0) {
        __builtin_amdgcn_s_setprio(2);
        wait_lgkm0();
        lds_inc(smem, L2_FLAG + 8, lane);
        wait_vm<0>();
        lds_inc(smem, L2_PUB, lane);
        while (lds_word(smem, L2_FLAG + 8) < 7u) __builtin_amdgcn_s_sleep(1);

        dma_range<EARLY, issued_before(EARLY)>(d);
        wait_vm<12 * (issued_before(EARLY) - 1 - EARLY)>();
        *(volatile LAS unsigned*)((LAS char*)smem + L2_FLAG) = (unsigned)(EARLY + 1);
        dma_run<EARLY + 1>(d);

        unsigned wo[5];
#pragma unroll
        for (int r = 0; r < 5; ++r) {
            const int L = r * 64 + lane, row = L / 10, c = L - row * 10;
            wo[r] = (unsigned)(row * (D_ * 2) + c * 16 - (r & 3) * 1024);
        }
        const int nu = b < 128 ? 2 : 1;
        for (int i = 0; i < nu; ++i) {
            const int u = b + 256 * i, nt2 = u / 48, kb = u - nt2 * 48;
            const char* g = w2 + (size_t)kb * (32 * D_ * 2) + nt2 * 160;
            const unsigned l0 = d.lw + L2_W2 + i * 5120, l1 = l0 + 4096;
            asm volatile("s_mov_b32 m0, %6\n\ts_nop 0\n\t"
                         "global_load_lds_dwordx4 %0, %5\n\t"
                         "global_load_lds_dwordx4 %1, %5 offset:1024\n\t"
                         "global_load_lds_dwordx4 %2, %5 offset:2048\n\t"
                         "global_load_lds_dwordx4 %3, %5 offset:3072\n\t"
                         "s_mov_b32 m0, %7\n\ts_nop 0\n\t"
                         "global_load_lds_dwordx4 %4, %5"
                         :: "v"(wo[0]), "v"(wo[1]), "v"(wo[2]), "v"(wo[3]), "v"(wo[4]), "s"(g), "s"(l0), "s"(l1) : "memory");
        }
        wait_vm<0>();
        const int gq = lane >> 4;
        const unsigned ra = L2_W2 + (16 * (gq >> 1) + 4 * (gq & 1) + ((lane & 15) >> 2)) * 160 + (lane & 3) * 8;
        for (int i = 0; i < nu; ++i) {
            char* dst = ws + WS_W2T + (size_t)(b + 256 * i) * 5120 + lane * 16;
#pragma unroll
            for (int j = 0; j < 5; ++j) {
                s16x4 lo = __builtin_amdgcn_ds_read_tr16_b64_v4i16((LAS s16x4*)((LAS char*)smem + ra + i * 5120 + j * 32));
                s16x4 hi = __builtin_amdgcn_ds_read_tr16_b64_v4i16((LAS s16x4*)((LAS char*)smem + ra + i * 5120 + j * 32 + 8 * 160));
                st16_wt(dst + j * 1024, __builtin_bit_cast(u32x4, __builtin_shufflevector(lo, hi, 0, 1, 2, 3, 4, 5, 6, 7)));
            }
        }

    } else if (wv >= 4) {
        __builtin_amdgcn_s_setprio(3);
        const int mw = wv - 4;
        const int h = lane >> 5, r32 = lane & 31;
        const int l16 = lane * 16;
        const __amdgpu_buffer_rsrc_t xr = __builtin_amdgcn_make_buffer_rsrc((void*)(ws + WS_XN + (size_t)mt * (20 * 8192) + mw * 1024), 0, 20 * 8192, 0x00020000);
        if (wv == 4) {
            const unsigned tag = lds_word(smem, L2_TAG);
            const unsigned* tp = sync + 1024 + mt * 64 + lane;
            __builtin_amdgcn_s_sleep(X_WAIT);
            for (unsigned spin = 0; spin < (1u << 20); ++spin) {
                unsigned f;
                asm volatile("global_load_dword %0, %1, off sc1\n\ts_waitcnt vmcnt(0)" : "=&v"(f) : "v"(tp) : "memory");
                if (__builtin_amdgcn_ballot_w64(f != tag && (lane & 3) < 2) == 0ull) break;
                __builtin_amdgcn_s_sleep(10);
            }
            *(volatile LAS unsigned*)((LAS char*)smem + L2_FLAG + 4) = 1u;
        } else {
            wait_vm<0>();
            lds_inc(smem, L2_FLAG + 8, lane);
            while (lds_word(smem, L2_FLAG + 4) == 0u) __builtin_amdgcn_s_sleep(1);
        }

        u32x4 xf[5][2];
#pragma unroll
        for (int st = 0; st < 5; ++st) {
            xf[st][0] = __builtin_amdgcn_raw_buffer_load_b128(xr, l16, st * 8192, 16);
            xf[st][1] = __builtin_amdgcn_raw_buffer_load_b128(xr, l16, st * 8192 + 4096, 16);
        }
        f32x16 acc[6];
#pragma unroll
        for (int i = 0; i < 6; ++i)
#pragma unroll
            for (int r = 0; r < 16; ++r) acc[i][r] = 0.f;
        u32x4 wa[6], wb[6];
        unsigned rc = ((lane >> 4) & 1) * WGRP + (8 * h + ((lane & 15) >> 2)) * 192 + (lane & 3) * 8, rn = rc + 5 * SLOT;
        unsigned fa = L2_FLAG, da = L2_FLAG + 16 + mw * 4, dv = 0;
        PIN(rc); PIN(rn); PIN(fa); PIN(da); PIN(dv);
#define MM(i, w, x) asm volatile("v_mfma_f32_32x32x16_bf16 %0, %1, %2, %0" : "+v"(acc[i]) : "v"(w), "v"(x) : "memory")
        while (lds_word(smem, L2_FLAG + 8) < 7u) __builtin_amdgcn_s_sleep(1);

#pragma unroll
        for (int i = 0; i < 6; ++i) wa[i] = wfrag(smem, rc + i * 32);
#pragma unroll 1
        for (int io = 0; io < 3; ++io) {
#pragma unroll
            for (int ii = 0; ii < 5; ++ii) {
                const int it = io * 5 + ii;
                const unsigned cb = rc + ii * SLOT + 2 * WGRP, nb = ii < 4 ? rc + (ii + 1) * SLOT : rn;
                MM(0, wa[0], xf[ii][0]);
                wb[0] = wfrag(smem, cb);
                unsigned fl = *(const volatile LAS unsigned*)((LAS const char*)smem + fa);
                MM(1, wa[1], xf[ii][0]);
                wb[1] = wfrag(smem, cb + 32);
                MM(2, wa[2], xf[ii][0]);
                wb[2] = wfrag(smem, cb + 64);
                MM(3, wa[3], xf[ii][0]);
                wb[3] = wfrag(smem, cb + 96);
                asm volatile("v_add_u32 %0, 1, %0" : "+v"(dv));
                MM(4, wa[4], xf[ii][0]);
                wb[4] = wfrag(smem, cb + 128);
                {
                    unsigned m = __builtin_amdgcn_readfirstlane(fl);
                    const unsigned need = it == 14 ? KSTEPS : it + 2;
                    while (m < need) { __builtin_amdgcn_s_sleep(1); m = lds_word(smem, L2_FLAG); }
                }
                MM(5, wa[5], xf[ii][0]);
                wb[5] = wfrag(smem, cb + 160);
                MM(0, wb[0], xf[ii][1]);
                wa[0] = wfrag(smem, nb);
                xf[ii][0] = __builtin_amdgcn_raw_buffer_load_b128(xr, l16, (it + 5) * 8192, 16);
                MM(1, wb[1], xf[ii][1]);
                wa[1] = wfrag(smem, nb + 32);
                MM(2, wb[2], xf[ii][1]);
                wa[2] = wfrag(smem, nb + 64);
                MM(3, wb[3], xf[ii][1]);
                wa[3] = wfrag(smem, nb + 96);
                MM(4, wb[4], xf[ii][1]);
                wa[4] = wfrag(smem, nb + 128);
                MM(5, wb[5], xf[ii][1]);
                wa[5] = wfrag(smem, nb + 160);
                if ((it & 1) && io < 2) *(volatile LAS unsigned*)((LAS char*)smem + da) = dv;
                xf[ii][1] = __builtin_amdgcn_raw_buffer_load_b128(xr, l16, (it + 5) * 8192 + 4096, 16);
            }
            { const unsigned t = rc; rc = rn; rn = t; }

        }
        char* ht = ws + WS_HT + (size_t)((((mt * 2 + (mw >> 1)) * 48 + 3 * nt) * 4 + 2 * (mw & 1) + (r32 >> 4)) * 64 + (r32 & 15) + 16 * h) * 16;
        u32x4 g0[9], g1[9];
#define TFRAG(i, n) wfrag(smem, rc + ((n) >> 1) * SLOT + ((n) & 1) * (2 * WGRP) + (i) * 32)
#pragma unroll
        for (int n = 1; n < 10; ++n) g0[n - 1] = TFRAG(0, n);
#pragma unroll
        for (int i = 0; i < 7; ++i) {
            float hh[8];
#pragma unroll
            for (int n = 0; n < 10; ++n) {
                if (i < 6) {
                    if (n == 0) MM(i, wa[i], xf[0][0]);
                    else if (i & 1) MM(i, g1[n - 1], xf[n >> 1][n & 1]);
                    else MM(i, g0[n - 1], xf[n >> 1][n & 1]);
                    if (i < 5 && n < 9) { if (i & 1) g0[n] = TFRAG(i + 1, n + 1); else g1[n] = TFRAG(i + 1, n + 1); }
                } else if (n == 0) {
                    asm volatile("s_nop 15\n\ts_nop 7" ::: "memory");
                }
                if (i > 0) {
                    if (n >= 1 && n <= 8) { hh[n - 1] = silu_f(acc[i - 1][n - 1]) * acc[i - 1][8 + n - 1]; PIN(hh[n - 1]); }
                    if (n == 9) {
                        const u32x4 o = {pack_bf16(hh[0], hh[1]), pack_bf16(hh[2], hh[3]), pack_bf16(hh[4], hh[5]), pack_bf16(hh[6], hh[7])};
                        st16_wt(ht + ((i - 1) >> 1) * 4096 + ((i - 1) & 1) * 512, o);
                    }
                }
                SB();
            }
        }

    } else {
        const int fw = wv - 1;
        const int q = xcd, g4 = fw * 32 + idx;
        wait_lgkm0();
        lds_inc(smem, L2_FLAG + 8, lane);
        wait_vm<0>();
        lds_inc(smem, L2_PUB, lane);
        if (g4 >= NG4) return;
        if (fw == 0) {
            const unsigned tag = lds_word(smem, L2_TAG);
            while (lds_word(smem, L2_PUB) < 4u) __builtin_amdgcn_s_sleep(1);
            if (lane < 8) st4_wt(sync + 2048 + lane * 256 + b, tag);

        }
        const int t = lane >> 2;
        cf tw[16];
#pragma unroll
        for (int a = 1; a < 16; ++a) { tw[a] = twid((float)(t * a) * (1.0f / 256.0f)); PIN(tw[a].x); PIN(tw[a].y); }
        SB();
        if (fw == 0) {
            const unsigned tag = lds_word(smem, L2_TAG);
            __builtin_amdgcn_s_sleep(P1_WAIT);
            const unsigned* tp = sync + 2048 + xcd * 256 + lane * 4;
            for (unsigned spin = 0; spin < (1u << 20); ++spin) {
                u32x4 f;
                asm volatile("global_load_dwordx4 %0, %1, off sc1\n\ts_waitcnt vmcnt(0)" : "=&v"(f) : "v"(tp) : "memory");
                if (__builtin_amdgcn_ballot_w64(f.x != tag || f.y != tag || f.z != tag || f.w != tag) == 0ull) break;
                __builtin_amdgcn_s_sleep(16);
            }
            *(volatile LAS unsigned*)((LAS char*)smem + L2_GO) = 1u;
            if (b == 0 && lane == 0) st4_wt(sync, tag);
        } else {
            while (lds_word(smem, L2_GO) == 0u) __builtin_amdgcn_s_sleep(4);
        }

        const int eo = (t * 8 + q) * TP + 4 * g4 + (lane & 3);
        const __amdgpu_buffer_rsrc_t tr = __builtin_amdgcn_make_buffer_rsrc((void*)(ws + WS_T16), 0, 2048 * TP * 4, 0x00020000);
        const __amdgpu_buffer_rsrc_t vr = __builtin_amdgcn_make_buffer_rsrc((void*)(ws + WS_T), 0, 2048 * TP * 4, 0x00020000);
        const __amdgpu_buffer_rsrc_t wtr = __builtin_amdgcn_make_buffer_rsrc((void*)(ws + WS_WT + (size_t)(q * NG4 + g4) * 4096), 0, 4096, 0x00020000);
        const __amdgpu_buffer_rsrc_t wmr = __builtin_amdgcn_make_buffer_rsrc((void*)(ws + WS_WMAX), 0, 3072, 0x00020000);
        unsigned tv[16];
        u32x4 wq[4];
#pragma unroll
        for (int i = 0; i < 16; ++i) tv[i] = __builtin_amdgcn_raw_buffer_load_b32(tr, eo * 4, i * (128 * TP * 4), 16);
#pragma unroll
        for (int i = 0; i < 4; ++i) wq[i] = __builtin_amdgcn_raw_buffer_load_b128(wtr, lane * 16, i * 1024, 16);
        const WMax wm = {__builtin_amdgcn_raw_buffer_load_b128(wmr, lane * 16, 0, 16), __builtin_amdgcn_raw_buffer_load_b128(wmr, lane * 16, 1024, 16), __builtin_amdgcn_raw_buffer_load_b128(wmr, lane * 16, 2048, 16)};
        SB();
        char* fs = smem + L2_F + fw * FSCR;
        const float vs = __builtin_bit_cast(float, (unsigned)(254 - vscale_exp(wm)) << 23);
        cf v[16];
#pragma unroll
        for (int i = 0; i < 16; ++i) { const f16x2 hv = __builtin_bit_cast(f16x2, tv[i]); v[i] = {(float)hv.x, (float)hv.y}; }

        fft16_a<-1>(v); fft16_t<-1>(v); fft16_b<-1>(v);
        lds_st(fs, lane, v[0]);
#pragma unroll
        for (int a = 1; a < 16; ++a) lds_st(fs, a * 68 + lane, cmul(v[a], tw[a]));
        wave_fence();
#pragma unroll
        for (int i = 0; i < 16; ++i) v[i] = lds_ld(fs, t * 68 + i * 4 + (lane & 3));
        fft16_a<-1>(v); fft16_t<-1>(v); fft16_b<-1>(v);
#pragma unroll
        for (int i = 0; i < 16; ++i) { const unsigned w = wq[i >> 2][i & 3]; v[i] = cmul(v[i], cf{bf_lo(w), bf_hi(w)}); }
        if (fw < 2) {
            char* sp = ws + WS_S + (size_t)b * 10240 + fw * 5120 + lane * 16;
#pragma unroll
            for (int i = 0; i < 5; ++i) st16_wt(sp + i * 1024, u32x4{CANARY, CANARY, CANARY, CANARY});
        }
        fft16_a<1>(v); fft16_t<1>(v); fft16_b<1>(v);
        wave_fence();
        lds_st(fs, t * 68 + (lane & 3), v[0]);
#pragma unroll
        for (int i = 1; i < 16; ++i) lds_st(fs, t * 68 + i * 4 + (lane & 3), cmulc(v[i], tw[i]));
        wave_fence();
#pragma unroll
        for (int a = 0; a < 16; ++a) v[a] = lds_ld(fs, a * 68 + lane);
        fft16_a<1>(v); fft16_t<1>(v); fft16_b<1>(v);
#pragma unroll
        for (int i = 0; i < 16; ++i)
            __builtin_amdgcn_raw_buffer_store_b32(__builtin_bit_cast(unsigned, f16x2{(_Float16)(v[i].x * vs), (_Float16)(v[i].y * vs)}), vr, eo * 4, i * (128 * TP * 4), 0);

    }
}

constexpr int RPITCH = 336;
constexpr int L3_RED = 0;
constexpr int L3_A = 4 * 64 * RPITCH;
constexpr int L3_B = L3_A + 4 * 720 * 8;
constexpr int L3_CNT = L3_B + 4 * 656 * 8;
constexpr int K3_LDS = L3_CNT + 64;
#define GO_STEP 9

DEV void helper_barrier(char* smem, int lane, unsigned target) {
    wait_lgkm0();
    LAS unsigned* cnt = (LAS unsigned*)((LAS char*)smem + L3_CNT);
    if (lane == 0) __hip_atomic_fetch_add(cnt, 1u, __ATOMIC_RELAXED, __HIP_MEMORY_SCOPE_WORKGROUP);
    while (__hip_atomic_load(cnt, __ATOMIC_RELAXED, __HIP_MEMORY_SCOPE_WORKGROUP) < target) __builtin_amdgcn_s_sleep(1);
    asm volatile("" ::: "memory");
}
#define MFMA16(a, b, c) __builtin_amdgcn_mfma_f32_16x16x32_bf16(__builtin_bit_cast(bf16x8, a), __builtin_bit_cast(bf16x8, b), c, 0, 0, 0)

extern "C" __global__ NOPK __launch_bounds__(768, 3) void k3_kernel(const char* __restrict__ x, char* __restrict__ ws, char* __restrict__ out) {
    extern __shared__ __attribute__((aligned(16))) char smem[];
    const int b = blockIdx.x, tid = threadIdx.x, lane = tid & 63;
    const int wv = __builtin_amdgcn_readfirstlane(tid >> 6);
    const int xcd = b & 7, idx = b >> 3;
    const int mt2 = (xcd & 3) * 8 + (idx & 7), nt2 = (xcd >> 2) * 4 + (idx >> 3);

    if (tid < 8) *(LAS unsigned*)((LAS char*)smem + L3_CNT + tid * 4) = 0u;
    wait_lgkm0();
    bar();
    if (wv >= 8) {
        __builtin_amdgcn_s_setprio(2);
        const int gw = wv - 8;
        const char* hb = ws + WS_HT + (size_t)(mt2 * 48 + 12 * gw) * 4096;
        const char* wb = ws + WS_W2T + (size_t)(nt2 * 48 + 12 * gw) * 5120;
        const int l16 = lane * 16;
        const __amdgpu_buffer_rsrc_t hr = __builtin_amdgcn_make_buffer_rsrc((void*)hb, 0, 12 * 4096, 0x00020000);
        const __amdgpu_buffer_rsrc_t wr_ = __builtin_amdgcn_make_buffer_rsrc((void*)wb, 0, 12 * 5120, 0x00020000);
#define HLD(st, tb) __builtin_amdgcn_raw_buffer_load_b128(hr, l16 + (tb) * 1024, (st) * 4096, 0)
#define WLD(st, j) __builtin_amdgcn_raw_buffer_load_b128(wr_, l16 + ((j) & 3) * 1024, (st) * 5120 + ((j) >> 2) * 4096, 0)

        u32x4 hf[2][4], wf[2][5];
#pragma unroll
        for (int st = 0; st < 2; ++st) {
#pragma unroll
            for (int tb = 0; tb < 4; ++tb) hf[st][tb] = HLD(st, tb);
#pragma unroll
            for (int j = 0; j < 5; ++j) wf[st][j] = WLD(st, j);
            SB();
        }
        f32x4 acc[5][4];
#pragma unroll
        for (int j = 0; j < 5; ++j)
#pragma unroll
            for (int tb = 0; tb < 4; ++tb) acc[j][tb] = f32x4{0.f, 0.f, 0.f, 0.f};
        char* rp = smem + L3_RED + gw * (64 * RPITCH) + (lane & 15) * RPITCH + (lane >> 4) * 16;
        SB();
#pragma unroll
        for (int st = 0; st < 12; ++st) {
            const int sl = st & 1;
            const bool more = st + 2 < 12;
#pragma unroll
            for (int j = 0; j < 4; ++j) {
#pragma unroll
                for (int tb = 0; tb < 4; ++tb) acc[j][tb] = MFMA16(wf[sl][j], hf[sl][tb], acc[j][tb]);
                SB();
                if (more) wf[sl][j] = WLD(st + 2, j);
                if (st == 11 && j > 0) {
#pragma unroll
                    for (int tb = 0; tb < 4; ++tb) *(LAS f32x4*)((LAS char*)rp + tb * (16 * RPITCH) + (j - 1) * 64) = acc[j - 1][tb];
                }
                SB();
            }
#pragma unroll
            for (int tb = 0; tb < 4; ++tb) {
                acc[4][tb] = MFMA16(wf[sl][4], hf[sl][tb], acc[4][tb]);
                SB();
                if (more) hf[sl][tb] = HLD(st + 2, tb);
                SB();
            }
            if (more) wf[sl][4] = WLD(st + 2, 4);
            if (st == GO_STEP) *(volatile LAS unsigned*)((LAS char*)smem + L3_CNT + 16 + gw * 4) = 1u;
            SB();
        }

#pragma unroll
        for (int j = 3; j < 5; ++j)
#pragma unroll
            for (int tb = 0; tb < 4; ++tb) *(LAS f32x4*)((LAS char*)rp + tb * (16 * RPITCH) + j * 64) = acc[j][tb];
        wait_lgkm0();

        bar();

    } else {
        __builtin_amdgcn_s_setprio(3);
        const int s0 = s0_of(b);
        const f16x2* V = (const f16x2*)(ws + WS_T) + (size_t)s0 * (8 * TP);
        char* sg = ws + WS_S;
        f16x2 u[10], u2[10], e0, e1;
        const WMax wm = vscale_load(ws, lane);
        if (wv < 4) {
            const int qa = wv, qb = wv == 0 ? 0 : 8 - wv;
#pragma unroll
            for (int bb = 0; bb < 5; ++bb) u[bb] = V[qa * TP + lane + 64 * bb];
#pragma unroll
            for (int bb = 5; bb < 10; ++bb) u[bb] = V[qb * TP + 640 - lane - 64 * bb];
            if (wv == 0) {
#pragma unroll
                for (int bb = 0; bb < 5; ++bb) u2[bb] = V[4 * TP + lane + 64 * bb];
#pragma unroll
                for (int bb = 5; bb < 10; ++bb) u2[bb] = V[4 * TP + 640 - lane - 64 * bb];
            } else {
                e0 = V[qb * TP];
                e1 = V[qa * TP + 320];
            }
        }
        SB();
        __builtin_amdgcn_s_setprio(0);

        if (wv < 5) {
            const int qr = tid / 80, r = tid - qr * 80;
            cf t80[10], t640[8];
#pragma unroll
            for (int t = 1; t < 10; ++t) { t80[t] = twid((float)((lane >> 3) * t) * (1.0f / 80.0f)); PIN(t80[t].x); PIN(t80[t].y); }
#pragma unroll
            for (int a = 1; a < 8; ++a) { t640[a] = twid((float)(r * a) * (1.0f / 640.0f)); PIN(t640[a].x); PIN(t640[a].y); }
            cf twq = twid((float)(s0 * (lane & 7)) * (1.0f / 2048.0f));
            SB();

            char* bufA = smem + L3_A;
            char* bufB = smem + L3_B;
            if (wv < 4) {
                const int ve = vscale_exp(wm);
                const float NORM = (1.0f / (2048.0f * 640.0f)) * __builtin_bit_cast(float, (unsigned)ve << 23);
                cf f[10];
                if (wv == 0) {
                    const cf w4 = {rdlane(twq.x, 4) * NORM, rdlane(twq.y, 4) * NORM};
#pragma unroll
                    for (int bb = 0; bb < 10; ++bb) {
                        cf A = {(float)u[bb].x * NORM, (float)u[bb].y * NORM}, B = cmulc(cf{(float)u2[bb].x, (float)u2[bb].y}, w4);
                        if (bb == 0 || bb == 5) { if (lane == 0) { A.y = 0.f; B.y = 0.f; } }
                        f[bb] = bb < 5 ? cf{A.x - B.y, A.y + B.x} : cf{A.x + B.y, B.x - A.y};
                    }
                } else {
                    const float N2 = 2.0f * NORM;
                    const cf wa = {rdlane(twq.x, wv) * N2, rdlane(twq.y, wv) * N2}, wb = {rdlane(twq.x, 8 - wv) * N2, rdlane(twq.y, 8 - wv) * N2};
#pragma unroll
                    for (int bb = 0; bb < 5; ++bb) f[bb] = cmulc(cf{(float)u[bb].x, (float)u[bb].y}, wa);
#pragma unroll
                    for (int bb = 5; bb < 10; ++bb) { cf t = cmulc(cf{(float)u[bb].x, (float)u[bb].y}, wb); f[bb] = cf{t.x, -t.y}; }
                    const cf g0 = cmulc(cf{(float)e0.x, (float)e0.y}, wb), g1 = cmulc(cf{(float)e1.x, (float)e1.y}, wa);
                    if (lane == 0) {
                        f[0] = cf{0.5f * (f[0].x + g0.x), 0.5f * (f[0].y - g0.y)};
                        f[5] = cf{0.5f * (f[5].x + g1.x), 0.5f * (f[5].y + g1.y)};
                    }
                }

                fft10<1>(f);
                lds_st(bufA, wv * 720 + lane, f[0]);
#pragma unroll
                for (int t = 1; t < 10; ++t) lds_st(bufA, wv * 720 + t * 72 + lane, cmulc(f[t], t80[t]));
            }

            helper_barrier(smem, lane, 5);

            cf a8[8];
            {
                const int base = qr * 720 + (r >> 3) * 72 + (r & 7);
#pragma unroll
                for (int i = 0; i < 8; ++i) a8[i] = lds_ld(bufA, base + 8 * i);
                fft8<1>(a8);
                const int ob = qr * 656 + (r & 7) * 82 + (r >> 3);
#pragma unroll
                for (int i = 0; i < 8; ++i) lds_st(bufB, ob + 10 * i, a8[i]);
            }

            helper_barrier(smem, lane, 10);

            {
                a8[0] = lds_ld(bufB, qr * 656 + r);
#pragma unroll
                for (int a = 1; a < 8; ++a) a8[a] = cmulc(lds_ld(bufB, qr * 656 + a * 82 + r), t640[a]);
                fft8<1>(a8);
#pragma unroll
                for (int i = 0; i < 8; ++i) lds_st(bufA, qr * 720 + r + 80 * i, a8[i]);
            }
            helper_barrier(smem, lane, 15);

            {
                f32x4 p[4];
#pragma unroll
                for (int q = 0; q < 4; ++q) p[q] = *(const LAS f32x4*)((LAS const char*)bufA + (q * 720 + 2 * tid) * 8);
                float y[2][8];
#pragma unroll
                for (int e = 0; e < 2; ++e) {
                    const float P0 = p[0][2 * e], P4 = p[0][2 * e + 1];
                    const float A = P0 + P4, B = P0 - P4;
                    const float c0 = A + p[2][2 * e], c2 = A - p[2][2 * e], c1 = B - p[2][2 * e + 1], c3 = B + p[2][2 * e + 1];
                    const float o0 = p[1][2 * e] + p[3][2 * e], o2 = p[3][2 * e + 1] - p[1][2 * e + 1];
                    const float uu = p[1][2 * e] - p[3][2 * e], vv = p[1][2 * e + 1] + p[3][2 * e + 1];
                    const float o1 = uu - vv, o3 = -uu - vv;
                    y[e][0] = c0 + o0; y[e][4] = c0 - o0;
                    y[e][2] = c2 + o2; y[e][6] = c2 - o2;
                    y[e][1] = c1 + RH * o1; y[e][5] = c1 - RH * o1;
                    y[e][3] = c3 + RH * o3; y[e][7] = c3 - RH * o3;
                }
                char* stg = bufB + wv * 2048;
#pragma unroll
                for (int j = 0; j < 8; ++j) *(LAS unsigned*)((LAS char*)stg + j * 256 + lane * 4) = pack_bf16(y[0][j], y[1][j]);
                wave_fence();
                __builtin_amdgcn_s_setprio(3);
#pragma unroll
                for (int hh = 0; hh < 2; ++hh) {
                    const int j = 4 * hh + (lane >> 4);
                    u32x4 o = *(const LAS u32x4*)((LAS const char*)stg + hh * 1024 + lane * 16);
                    st16_wt(sg + (size_t)(s0 + 256 * j) * (D_ * 2) + wv * 256 + (lane & 15) * 16, o);
                }
            }
            __builtin_amdgcn_s_setprio(0);

        }
        const int r0 = tid / 10, c0 = tid - r0 * 10, p1 = 512 + (tid >> 2), r1 = p1 / 10, c1 = p1 - r1 * 10;
        const size_t eo0 = (size_t)(mt2 * 8 + (r0 >> 3) + 256 * (r0 & 7)) * (D_ * 2) + nt2 * 160 + c0 * 16;
        const size_t eo1 = (size_t)(mt2 * 8 + (r1 >> 3) + 256 * (r1 & 7)) * (D_ * 2) + nt2 * 160 + c1 * 16 + (tid & 3) * 4;
        SB();
        while (lds_min4(smem, L3_CNT + 16) == 0u) __builtin_amdgcn_s_sleep(2);
        __builtin_amdgcn_s_setprio(3);

        const u32x4 xa = *(const u32x4*)(x + eo0);
        const unsigned xb = *(const unsigned*)(x + eo1);
        u32x4 sa; unsigned sb;
        {
            const char* src0 = sg + eo0;
            const char* src1 = sg + eo1;
            for (unsigned spin = 0; spin < (1u << 20); ++spin) {
                asm volatile("global_load_dwordx4 %0, %2, off sc1\n\tglobal_load_dword %1, %3, off sc1\n\ts_waitcnt vmcnt(0)"
                             : "=&v"(sa), "=&v"(sb) : "v"(src0), "v"(src1) : "memory");
                const bool bad = sa.x == CANARY || sa.y == CANARY || sa.z == CANARY || sa.w == CANARY || sb == CANARY;
                if (__builtin_amdgcn_ballot_w64(bad) == 0ull) break;
                __builtin_amdgcn_s_sleep(8);
            }
        }

        bar();

        {
            const char* rp = smem + L3_RED + r0 * RPITCH + c0 * 32;
            const char* rq = smem + L3_RED + r1 * RPITCH + c1 * 32 + (tid & 3) * 8;
            f32x4 o0 = *(const LAS f32x4*)((LAS const char*)rp), o1 = *(const LAS f32x4*)((LAS const char*)rp + 16);
            f32x2 o2 = *(const LAS f32x2*)((LAS const char*)rq);
#pragma unroll
            for (int w = 1; w < 4; ++w) {
                o0 += *(const LAS f32x4*)((LAS const char*)rp + w * (64 * RPITCH));
                o1 += *(const LAS f32x4*)((LAS const char*)rp + w * (64 * RPITCH) + 16);
                o2 += *(const LAS f32x2*)((LAS const char*)rq + w * (64 * RPITCH));
            }
            const float o[8] = {o0.x, o0.y, o0.z, o0.w, o1.x, o1.y, o1.z, o1.w};
            u32x4 res;
#pragma unroll
            for (int i = 0; i < 4; ++i)
                res[i] = pack_bf16(bf_lo(xa[i]) + bf_lo(sa[i]) * silu_f(o[2 * i]), bf_hi(xa[i]) + bf_hi(sa[i]) * silu_f(o[2 * i + 1]));
            *(u32x4*)(out + eo0) = res;
            *(unsigned*)(out + eo1) = pack_bf16(bf_lo(xb) + bf_lo(sb) * silu_f(o2.x), bf_hi(xb) + bf_hi(sb) * silu_f(o2.y));
        }

    }
}

extern "C" void kernel_launch(void* const* d_in, const int* in_sizes, int n_in, void* d_out, int out_size, void* d_ws,
                              size_t ws_size, hipStream_t stream) {
    (void)in_sizes; (void)n_in; (void)out_size; (void)ws_size;
    char* ws = (char*)d_ws;
    (void)hipFuncSetAttribute((const void*)k12_kernel, hipFuncAttributeMaxDynamicSharedMemorySize, K2_LDS);
    (void)hipFuncSetAttribute((const void*)k3_kernel, hipFuncAttributeMaxDynamicSharedMemorySize, K3_LDS);
    k12_kernel<<<256, 512, K2_LDS, stream>>>((const unsigned*)d_in[0], (const char*)d_in[4], (const char*)d_in[5], (const char*)d_in[6],
                                             (const unsigned short*)d_in[1], (const unsigned short*)d_in[2], ws);
    k3_kernel<<<256, 768, K3_LDS, stream>>>((const char*)d_in[0], ws, (char*)d_out);
}
```
